```python
import torch

def _softmax(x, axis=-1):
    return torch.softmax(x, dim=axis)
def _gelu(x):
    return torch.nn.functional.gelu(x, approximate="tanh")
def _silu(x):
    return torch.nn.functional.silu(x)
def _sigmoid(x):
    return torch.sigmoid(x)
def _layernorm(x, axis=-1, eps=1e-5):
    m = x.mean(dim=axis, keepdim=True)
    v = x.var(dim=axis, keepdim=True, unbiased=False)
    return (x - m) / torch.sqrt(v + eps)
def _scatter_add(tgt, idx, src, axis=0):
    return tgt.clone().index_add_(axis, idx, src)
def _segment_sum(src, seg, n):
    out = torch.zeros((n,) + src.shape[1:], dtype=src.dtype)
    return out.index_add_(0, seg, src)
def _pad(x, pw):
    flat = tuple(v for pair in reversed(pw) for v in pair)
    return torch.nn.functional.pad(x, flat)

kernel_name = 'standalone__weight_only_int_omniquant__prompt'

def setup_inputs():

    _w = (lambda a: (lambda t: t.to(torch.bfloat16) if t.is_floating_point() else t)(torch.as_tensor(a)))
    B, S, D, F = 1, 2048, 1024, 3584
    bits = 8
    G = 32 if 32 > 0 else D
    D = (D // G) * G
    ng = D // G
    qmax = (1 << bits) - 1
    qw = torch.randint(0, qmax + 1, (D, F)).to(torch.int32)
    scales = (torch.abs((lambda *s: torch.randn(*s, dtype=torch.bfloat16) * 0.05)(ng, F)) * 0.05 + 0.01).to(float)
    zeros = torch.randint(0, qmax + 1, (ng, F)).to(float)
    act_scale = (torch.abs((lambda *s: torch.randn(*s, dtype=torch.bfloat16) * 0.05)(D)) * 0.5 + 0.5).to(float)
    return {
        "x": _w((lambda *s: torch.randn(*s, dtype=torch.bfloat16) * 0.05)(B, S, D)),
        "qw": _w(qw),
        "scales": _w(scales),
        "zeros": _w(zeros),
        "act_scale": _w(act_scale),
    }

def reference(x, qw, scales, zeros, act_scale):
    B, S, D = x.shape
    ng, F = scales.shape
    G = D // ng
    s = (lambda x, n, axis: torch.repeat_interleave(x, n, dim=axis))(scales, G, axis=0)
    z = (lambda x, n, axis: torch.repeat_interleave(x, n, dim=axis))(zeros, G, axis=0)
    w = ((lambda x, dt: x.to(dt))(qw, x.dtype) - z) * s
    xs = x * (lambda x, dt: x.to(dt))(act_scale, x.dtype)
    return torch.einsum("bsd,df->bsf", xs, w)
```

```cpp
#include <hip/hip_runtime.h>
#include <hip/hip_bf16.h>
typedef __attribute__((ext_vector_type(8))) short bf16x8;
typedef __attribute__((ext_vector_type(4))) float f32x4;
typedef unsigned short u16;
constexpr int M=2048,K=1024,N=3584;
__device__ __forceinline__ float bf2f(u16 v){return __uint_as_float(((unsigned)v)<<16);}
__device__ __forceinline__ u16 f2bf(float f){ __hip_bfloat16 h=__float2bfloat16(f); return *(u16*)&h;}
__global__ __launch_bounds__(256) void k_simple(const u16* x,const int* qw,const u16* sc,const u16* zr,const u16* as,u16* out){
  __shared__ u16 sA[64][40];
  __shared__ u16 sB[64][40];
  int bm=blockIdx.y*64,bn=blockIdx.x*64;
  int t=threadIdx.x,w=t>>6,l=t&63;
  int wm=(w>>1)*32,wn=(w&1)*32;
  f32x4 acc[2][2]={};
  for(int k0=0;k0<K;k0+=32){
    for(int i=t;i<64*32;i+=256){int r=i>>5,c=i&31; sA[r][c]=x[(bm+r)*K+k0+c];}
    for(int i=t;i<64*32;i+=256){int kk=i>>6,n=i&63; int g=(k0+kk)>>5;
      float s=bf2f(sc[g*N+bn+n]),z=bf2f(zr[g*N+bn+n]);
      float wv=fmaf((float)qw[(k0+kk)*N+bn+n],s,-z*s)*bf2f(as[k0+kk]);
      sB[n][kk]=f2bf(wv);}
    __syncthreads();
    for(int i=0;i<2;i++)for(int j=0;j<2;j++){
      bf16x8 a=*(bf16x8*)&sA[wm+i*16+(l&15)][(l>>4)*8];
      bf16x8 b=*(bf16x8*)&sB[wn+j*16+(l&15)][(l>>4)*8];
      acc[i][j]=__builtin_amdgcn_mfma_f32_16x16x32_bf16(a,b,acc[i][j],0,0,0);
    }
    __syncthreads();
  }
  for(int i=0;i<2;i++)for(int j=0;j<2;j++)for(int r=0;r<4;r++)
    out[(bm+wm+i*16+(l>>4)*4+r)*N+bn+wn+j*16+(l&15)]=f2bf(acc[i][j][r]);
}
extern "C" void kernel_launch(void* const* d_in,const int* in_sizes,int n_in,void* d_out,int out_size,void* d_ws,size_t ws_size,hipStream_t stream){
  k_simple<<<dim3(N/64,M/64),256,0,stream>>>((const u16*)d_in[0],(const int*)d_in[1],(const u16*)d_in[2],(const u16*)d_in[3],(const u16*)d_in[4],(u16*)d_out);
}
```

```cpp
#include <hip/hip_runtime.h>
#include <hip/hip_bf16.h>
typedef __attribute__((ext_vector_type(8))) short bf16x8;
typedef __attribute__((ext_vector_type(4))) float f32x4;
typedef unsigned short u16;
constexpr int M=2048,K=1024,N=3584;
__device__ __forceinline__ float bf2f(u16 v){return __uint_as_float(((unsigned)v)<<16);}
__device__ __forceinline__ u16 f2bf(float f){ __hip_bfloat16 h=__float2bfloat16(f); return *(u16*)&h;}
__global__ __launch_bounds__(256) void k_simple(const u16* x,const int* qw,const u16* sc,const u16* zr,const u16* as,u16* out){
  __shared__ u16 sA[64][40];
  __shared__ u16 sB[64][40];
  int bm=blockIdx.y*64,bn=blockIdx.x*64;
  int t=threadIdx.x,w=t>>6,l=t&63;
  int wm=(w>>1)*32,wn=(w&1)*32;
  f32x4 acc[2][2]={};
  for(int k0=0;k0<K;k0+=32){
    for(int i=t;i<64*32;i+=256){int r=i>>5,c=i&31; sA[r][c]=x[(bm+r)*K+k0+c];}
    for(int i=t;i<64*32;i+=256){int kk=i>>6,n=i&63; int g=(k0+kk)>>5;
      float s=bf2f(sc[g*N+bn+n]),z=bf2f(zr[g*N+bn+n]);
      float wv=fmaf((float)qw[(k0+kk)*N+bn+n],s,-z*s)*bf2f(as[k0+kk]);
      sB[n][kk]=f2bf(wv);}
    __syncthreads();
    for(int i=0;i<2;i++)for(int j=0;j<2;j++){
      bf16x8 a=*(bf16x8*)&sA[wm+i*16+(l&15)][(l>>4)*8];
      bf16x8 b=*(bf16x8*)&sB[wn+j*16+(l&15)][(l>>4)*8];
      acc[i][j]=__builtin_amdgcn_mfma_f32_16x16x32_bf16(a,b,acc[i][j],0,0,0);
    }
    __syncthreads();
  }
  for(int i=0;i<2;i++)for(int j=0;j<2;j++)for(int r=0;r<4;r++)
    out[(bm+wm+i*16+(l>>4)*4+r)*N+bn+wn+j*16+(l&15)]=f2bf(acc[i][j][r]);
}

__global__ void k_chase(const char* p,int cnt,int stride,int* sink){
  unsigned off=0; int acc=0;
  for(int i=0;i<cnt;i++){ int v; asm volatile("global_load_dword %0, %1, %2\n\ts_waitcnt vmcnt(0)":"=v"(v):"v"(off),"s"(p):"memory"); acc+=v; off=(unsigned)(i+1)*stride; }
  sink[0]=acc;
}
extern "C" void kernel_launch(void* const* d_in,const int* in_sizes,int n_in,void* d_out,int out_size,void* d_ws,size_t ws_size,hipStream_t stream){
  k_chase<<<1,1,0,stream>>>((const char*)d_in[1],2048,7168,(int*)d_ws);
  k_simple<<<dim3(N/64,M/64),256,0,stream>>>((const u16*)d_in[0],(const int*)d_in[1],(const u16*)d_in[2],(const u16*)d_in[3],(const u16*)d_in[4],(u16*)d_out);
}
```

```cpp
#define NTLOAD 1
#include <hip/hip_runtime.h>
#include <hip/hip_bf16.h>
using bf16 = __hip_bfloat16;
using bf16x8 = __attribute__((ext_vector_type(8))) short;
using f32x4 = __attribute__((ext_vector_type(4))) float;
constexpr int BM=256, BK=64, HALF=128, NXCD=8, WGM=8, HT=HALF*BK, SHM_B=8*HT*2;

__device__ __forceinline__ int lds_byte(int r, int c) {
  int st=(r>>4)*2+(c>>5), rr=r&15, cc=c&31, ob=rr*64+cc*2;
  return st*1024 + (ob ^ (((ob>>9)&1)<<5));
}
__device__ __forceinline__ void stage_rc(int b, int&R, int&C) {
  int st=b/1024, sb=b%1024, swz=sb^(((sb>>9)&1)<<5);
  R=(st>>1)*16+swz/64; C=(st&1)*32+(swz%64)/2;
}

__global__ __launch_bounds__(512,2)
void gemm_256sq_8phase(const bf16*A,const bf16*Bt,bf16*C,int M,int N,int K){
  extern __shared__ __attribute__((aligned(16))) bf16 shm[];
  #define SA(b,h) (shm+((b)*2+(h))*HT)
  #define SB(b,h) (shm+(4+(b)*2+(h))*HT)
  #define STAGE_X(T,P,BASE,br,kt) do{long _g=(long)(br)*K+(long)(kt)*BK; \
    for(int _i=0;_i<2;++_i){int _b=(T)*16+_i*8192;int _r,_c;stage_rc(_b,_r,_c); \
      __builtin_amdgcn_global_load_lds((const unsigned*)(BASE+_g+(long)_r*K+_c), \
        (unsigned*)((char*)(P)+_b),16,0,0);}}while(0)
  #define STAGE(P,BASE,br,kt) STAGE_X(threadIdx.x,P,BASE,br,kt)
  #define LDA(dst,b,h) for(int m=0;m<4;++m)for(int k=0;k<2;++k) \
    dst[m][k]=*reinterpret_cast<const bf16x8*>((char*)SA(b,h)+lds_byte(wr*64+m*16+fr,k*32+fq*8))
  #define LDB(dst,b,h) for(int n=0;n<2;++n)for(int k=0;k<2;++k) \
    dst[n][k]=*reinterpret_cast<const bf16x8*>((char*)SB(b,h)+lds_byte(wc*32+n*16+fr,k*32+fq*8))
  #define MMA(ai,bj,At,Bt) do{__builtin_amdgcn_s_setprio(1); \
    for(int m=0;m<4;++m)for(int n=0;n<2;++n)for(int k=0;k<2;++k) \
      acc[ai][bj][m][n]=__builtin_amdgcn_mfma_f32_16x16x32_bf16(At[m][k],Bt[n][k],acc[ai][bj][m][n],0,0,0); \
    __builtin_amdgcn_s_setprio(0);}while(0)
  #define WAIT_V(n) asm volatile("s_waitcnt vmcnt(" #n ")":::"memory")
  #define WAIT_L(n) asm volatile("s_waitcnt lgkmcnt(" #n ")":::"memory")
  #define BAR __builtin_amdgcn_s_barrier()
  #define SCHED __builtin_amdgcn_sched_barrier(0)

  int nM=M/BM,nN=N/BM,nwg=nM*nN,wgid=blockIdx.x;
  {int q=nwg/NXCD,r=nwg%NXCD,xcd=wgid%NXCD,off=wgid/NXCD;
   wgid=(xcd<r?xcd*(q+1):r*(q+1)+(xcd-r)*q)+off;}
  int nig=WGM*nN,gid=wgid/nig,fm=gid*WGM,gsz=min(nM-fm,WGM);
  int pm=fm+((wgid%nig)%gsz),pn=(wgid%nig)/gsz,brow=pm*BM,bcol=pn*BM;
  int wid=threadIdx.x>>6,lane=threadIdx.x&63,wr=wid>>2,wc=wid&3,fr=lane&15,fq=lane>>4;
  f32x4 acc[2][2][4][2]={};
  bf16x8 At[4][2],B0[2][2],B1[2][2];
  int nt=K/BK;
  STAGE(SB(0,0),Bt,bcol,0); STAGE(SA(0,0),A,brow,0);
  STAGE(SB(0,1),Bt,bcol+HALF,0); STAGE(SA(0,1),A,brow+HALF,0);
  if(wr==1)BAR;
  WAIT_V(4); BAR;
  STAGE(SB(1,0),Bt,bcol,1); STAGE(SA(1,0),A,brow,1); STAGE(SB(1,1),Bt,bcol+HALF,1);
  WAIT_V(6); BAR;
  for(int t=0;t<nt-2;t+=2){
    LDB(B0,0,0); SCHED; LDA(At,0,0); STAGE(SA(1,1),A,brow+HALF,t+1);
    WAIT_L(8); BAR; WAIT_L(0); MMA(0,0,At,B0); BAR; SCHED;
    LDB(B1,0,1); STAGE(SB(0,0),Bt,bcol,t+2);
    BAR; WAIT_L(0); MMA(0,1,At,B1); BAR;
    LDA(At,0,1); STAGE(SA(0,0),A,brow,t+2);
    BAR; WAIT_L(0); MMA(1,0,At,B0); BAR; SCHED;
    STAGE(SB(0,1),Bt,bcol+HALF,t+2);
    WAIT_V(6); BAR; MMA(1,1,At,B1); BAR;
    LDB(B0,1,0); SCHED; LDA(At,1,0); STAGE(SA(0,1),A,brow+HALF,t+2);
    WAIT_L(8); BAR; WAIT_L(0); MMA(0,0,At,B0); BAR; SCHED;
    LDB(B1,1,1); STAGE(SB(1,0),Bt,bcol,t+3);
    BAR; WAIT_L(0); MMA(0,1,At,B1); BAR;
    LDA(At,1,1); STAGE(SA(1,0),A,brow,t+3);
    BAR; WAIT_L(0); MMA(1,0,At,B0); BAR; SCHED;
    STAGE(SB(1,1),Bt,bcol+HALF,t+3);
    WAIT_V(6); BAR; MMA(1,1,At,B1); BAR;
  }
  int tz=threadIdx.x; asm volatile("":"+v"(tz)); wid=tz>>6; lane=tz&63; wr=wid>>2; wc=wid&3; fr=lane&15; fq=lane>>4;
  { LDB(B0,0,0); WAIT_V(0); LDA(At,0,0); STAGE_X(tz,SA(1,1),A,brow+HALF,nt-1);
    BAR; WAIT_L(0); MMA(0,0,At,B0); BAR;
    LDB(B1,0,1); BAR; WAIT_L(0); MMA(0,1,At,B1); BAR;
    LDA(At,0,1); WAIT_V(4); BAR; WAIT_L(0); MMA(1,0,At,B0); MMA(1,1,At,B1); BAR; }
  { LDB(B0,1,0); LDA(At,1,0); WAIT_V(2); BAR; WAIT_L(0); MMA(0,0,At,B0); BAR;
    LDB(B1,1,1); WAIT_V(0); BAR; WAIT_L(0); MMA(0,1,At,B1); BAR;
    LDA(At,1,1); BAR; WAIT_L(0); MMA(1,0,At,B0); MMA(1,1,At,B1); BAR; }
  if(wr==0)BAR;
  for(int ai=0;ai<2;++ai)for(int bj=0;bj<2;++bj)for(int m=0;m<4;++m)for(int n=0;n<2;++n)for(int j=0;j<4;++j)
    C[(long)(brow+ai*HALF+wr*64+m*16+fq*4+j)*N+(bcol+bj*HALF+wc*32+n*16+fr)]=__float2bfloat16(acc[ai][bj][m][n][j]);
}

typedef unsigned short u16;
typedef __attribute__((ext_vector_type(4))) int i32x4;
typedef __attribute__((ext_vector_type(4))) unsigned u32x4;
__device__ __forceinline__ float bf2f(u16 v){return __uint_as_float(((unsigned)v)<<16);}
__device__ __forceinline__ unsigned pk(float a,float b){ unsigned r; asm("v_cvt_pk_bf16_f32 %0, %1, %2":"=v"(r):"v"(a),"v"(b)); return r;}
__global__ __launch_bounds__(448) void k_deq(const int* qw,const u16* sc,const u16* zr,const u16* as,u16* wt){
  constexpr int K=1024,N=3584;
  int wv=blockIdx.x*7+(threadIdx.x>>6), l=threadIdx.x&63;
  int kb=wv/112, nb=wv%112; int kc=l>>3,cq=l&7;
  int k0=kb*64+kc*8, n=nb*32+cq*4;
  i32x4 q[8];
  #pragma unroll
  for(int j=0;j<8;j++){ const i32x4* p=(const i32x4*)(qw+(size_t)(k0+j)*N+n);
    q[j]=__builtin_nontemporal_load(p);
  }
  int g=k0>>5; float s[4],c[4],a[8];
  #pragma unroll
  for(int r=0;r<4;r++){ s[r]=bf2f(sc[g*N+n+r]); c[r]=-bf2f(zr[g*N+n+r])*s[r]; }
  #pragma unroll
  for(int j=0;j<8;j++) a[j]=bf2f(as[k0+j]);
  #pragma unroll
  for(int r=0;r<4;r++){ u32x4 o;
    #pragma unroll
    for(int j=0;j<4;j++) o[j]=pk(fmaf((float)q[2*j][r],s[r],c[r])*a[2*j],fmaf((float)q[2*j+1][r],s[r],c[r])*a[2*j+1]);
    *(u32x4*)(wt+(size_t)(n+r)*K+k0)=o; }
}
extern "C" void kernel_launch(void* const* d_in,const int* in_sizes,int n_in,void* d_out,int out_size,void* d_ws,size_t ws_size,hipStream_t stream){
  k_deq<<<256,448,0,stream>>>((const int*)d_in[1],(const u16*)d_in[2],(const u16*)d_in[3],(const u16*)d_in[4],(u16*)d_ws);
  hipFuncSetAttribute((const void*)gemm_256sq_8phase,hipFuncAttributeMaxDynamicSharedMemorySize,SHM_B);
  gemm_256sq_8phase<<<8*14,512,SHM_B,stream>>>((const bf16*)d_in[0],(const bf16*)d_ws,(bf16*)d_out,2048,3584,1024);
}
```

```cpp
#include <hip/hip_runtime.h>
typedef unsigned short u16;
__global__ __launch_bounds__(512,2) void wq_gemm(const u16* x,const int* qw,const u16* sc,const u16* zr,const u16* as,u16* out,unsigned* ws){
  __shared__ __attribute__((aligned(16))) char lds[147456];
  const int w=__builtin_amdgcn_readfirstlane(threadIdx.x>>6);
  const unsigned long long ka=(unsigned long long)__builtin_amdgcn_kernarg_segment_ptr();
  const unsigned ldsa=(unsigned)(size_t)lds;
  asm volatile(
    "s_load_dwordx2 s[20:21], %0, 8\n\t"
    "s_load_dwordx2 s[16:17], %0, 0\n\t"
    "s_load_dwordx2 s[24:25], %0, 16\n\t"
    "s_load_dwordx2 s[28:29], %0, 24\n\t"
    "s_load_dwordx2 s[36:37], %0, 32\n\t"
    "s_load_dwordx2 s[44:45], %0, 48\n\t"
    "s_load_dwordx2 s[40:41], %0, 40\n\t"
    "v_mbcnt_lo_u32_b32 v255, -1, 0\n\t"
    "v_mbcnt_hi_u32_b32 v255, -1, v255\n\t"
    "s_lshl_b32 s14, %2, 11\n\t"
    "s_getpc_b64 s[12:13]\n\t"
    "Lpd%=:\n\t"
    "v_lshl_add_u32 v252, v255, 7, s14\n\t"
    "s_cmp_lt_u32 %1, 224\n\t"
    "s_cselect_b32 s14, Lend%=-Lpd%=, 0\n\t"
    "v_cmp_gt_u32 vcc, s14, v252\n\t"
    "s_nop 1\n\t"
    "s_and_b32 exec_lo, vcc_lo, 0xffff\n\t"
    "s_mov_b32 exec_hi, 0\n\t"
    "s_cbranch_execz Lpe%=\n\t"
    "global_load_dword v253, v252, s[12:13]\n\t"
    "Lpe%=:\n\t"
    "s_mov_b64 exec, -1\n\t"
    "s_and_b32 s67, %1, 7\n\t"
    "s_lshr_b32 s68, %1, 3\n\t"
    "s_and_b32 s66, %2, 3\n\t"
    "s_lshr_b32 s55, s67, 1\n\t"
    "s_mul_i32 s70, s55, 7\n\t"
    "s_lshr_b32 s57, s68, 2\n\t"
    "s_add_u32 s70, s70, s57\n\t"
    "s_and_b32 s54, s67, 1\n\t"
    "s_lshl_b32 s69, s54, 2\n\t"
    "s_mov_b32 s22, 14680064\n\t"
    "s_mov_b32 s23, 0x20000\n\t"
    "s_mov_b32 s18, 4194304\n\t"
    "s_mov_b32 s19, 0x20000\n\t"
    "s_mov_b32 s26, 229376\n\t"
    "s_mov_b32 s27, 0x20000\n\t"
    "s_mov_b32 s30, 229376\n\t"
    "s_mov_b32 s31, 0x20000\n\t"
    "s_mov_b32 s38, 2048\n\t"
    "s_mov_b32 s39, 0x20000\n\t"
    "s_mov_b32 s46, 7405568\n\t"
    "s_mov_b32 s47, 0x20000\n\t"
    "s_mov_b32 s42, 14680064\n\t"
    "s_mov_b32 s43, 0x20000\n\t"
    "s_cmp_ge_u32 s68, 28\n\t"
    "s_cbranch_scc1 Lhelp%=\n\t"
    "s_and_b32 s56, s68, 3\n\t"
    "s_add_u32 s69, s69, s56\n\t"
    "s_lshl_b32 s71, s69, 1\n\t"
    "s_lshl_b32 s84, s70, 2\n\t"
    "s_add_u32 s84, s84, s66\n\t"
    "s_lshl_b32 s84, s84, 6\n\t"
    "v_lshlrev_b32 v212, 2, v255\n\t"
    "s_mov_b32 s50, 64\n\t"
    "s_mov_b32 s51, 0x20000\n\t"
    "s_cmp_ge_u32 %2, 4\n\t"
    "s_cbranch_scc1 Lcons%=\n\t"
    "v_lshrrev_b32 v226, 3, v255\n\t"
    "v_lshlrev_b32 v226, 11, v226\n\t"
    "v_lshrrev_b32 v227, 4, v255\n\t"
    "v_and_b32 v228, 7, v255\n\t"
    "v_xor_b32 v227, v227, v228\n\t"
    "v_lshl_add_u32 v216, v227, 4, v226\n\t"
    "v_xor_b32 v217, 64, v216\n\t"
    "v_add_u32 v218, 30720, v216\n\t"
    "v_add_u32 v219, 46080, v217\n\t"
    "v_add_u32 v220, 65536, v216\n\t"
    "v_add_u32 v221, 80896, v217\n\t"
    "v_add_u32 v222, 96256, v216\n\t"
    "v_add_u32 v223, 111616, v217\n\t"
    "v_add_u32 v217, 15360, v217\n\t"
    "s_lshl_b32 s72, s69, 2\n\t"
    "s_add_u32 s72, s72, s66\n\t"
    "s_lshl_b32 s72, s72, 17\n\t"
    "s_lshl_b32 s58, s71, 7\n\t"
    "s_add_u32 s58, s58, s72\n\t"
    "s_lshl_b32 s78, s66, 13\n\t"
    "s_mov_b32 s63, s78\n\t"
    "s_waitcnt lgkmcnt(0)\n\t"
    "s_and_b32 s17, s17, 0xffff\n\t"
    "s_mov_b32 m0, s63\n\t"
    "s_nop 0\n\t"
    "buffer_load_dwordx4 v216, s[16:19], s58 offen lds\n\t"
    "buffer_load_dwordx4 v217, s[16:19], s58 offen offset:1024 lds\n\t"
    "buffer_load_dwordx4 v218, s[16:19], s58 offen offset:2048 lds\n\t"
    "buffer_load_dwordx4 v219, s[16:19], s58 offen offset:3072 lds\n\t"
    "s_add_u32 m0, s63, 4096\n\t"
    "s_nop 0\n\t"
    "buffer_load_dwordx4 v220, s[16:19], s58 offen lds\n\t"
    "buffer_load_dwordx4 v221, s[16:19], s58 offen offset:1024 lds\n\t"
    "buffer_load_dwordx4 v222, s[16:19], s58 offen offset:2048 lds\n\t"
    "buffer_load_dwordx4 v223, s[16:19], s58 offen offset:3072 lds\n\t"
    "s_add_u32 s48, s44, s84\n\t"
    "s_addc_u32 s49, s45, 0\n\t"
    "s_and_b32 s49, s49, 0xffff\n\t"
    "buffer_load_dword v213, v212, s[48:51], 0 offen sc1\n\t"
    "s_branch Lprod%=\n\t"
    "Lcons%=:\n\t"
    "s_setprio 3\n\t"
    "v_lshrrev_b32 v224, 5, v255\n\t"
    "v_and_b32 v225, 31, v255\n\t"
    "s_lshl_b32 s54, s66, 4\n\t"
    "v_lshl_add_u32 v226, v224, 3, s54\n\t"
    "v_lshlrev_b32 v209, 1, v226\n\t"
    "v_mul_u32_u24 v227, 14336, v226\n\t"
    "v_lshl_add_u32 v200, v225, 4, v227\n\t"
    "v_add_u32 v201, 14336, v200\n\t"
    "v_add_u32 v202, 28672, v200\n\t"
    "v_add_u32 v203, 43008, v200\n\t"
    "v_add_u32 v204, 57344, v200\n\t"
    "v_add_u32 v205, 71680, v200\n\t"
    "v_add_u32 v206, 86016, v200\n\t"
    "v_add_u32 v207, 100352, v200\n\t"
    "v_lshlrev_b32 v208, 3, v225\n\t"
    "s_lshl_b32 s73, s70, 9\n\t"
    "s_lshr_b32 s54, s66, 1\n\t"
    "s_mul_i32 s54, s54, 7168\n\t"
    "s_lshl_b32 s74, s70, 8\n\t"
    "s_add_u32 s74, s74, s54\n\t"
    "s_mul_i32 s59, s71, 917504\n\t"
    "s_add_u32 s59, s59, s73\n\t"
    "s_mul_i32 s60, s71, 14336\n\t"
    "s_add_u32 s60, s60, s74\n\t"
    "s_lshl_b32 s61, s71, 7\n\t"
    "s_waitcnt lgkmcnt(0)\n\t"
    "s_and_b32 s25, s25, 0xffff\n\t"
    "s_and_b32 s29, s29, 0xffff\n\t"
    "s_and_b32 s37, s37, 0xffff\n\t"
    "s_and_b32 s21, s21, 0xffff\n\t"
    "buffer_load_dwordx2 v[192:193], v208, s[24:27], s60 offen\n\t"
    "buffer_load_dwordx2 v[194:195], v208, s[28:31], s60 offen\n\t"
    "buffer_load_dwordx4 v[196:199], v209, s[36:39], s61 offen\n\t"
    "buffer_load_dwordx4 v[128:131], v200, s[20:23], s59 offen nt\n\t"
    "buffer_load_dwordx4 v[132:135], v201, s[20:23], s59 offen nt\n\t"
    "buffer_load_dwordx4 v[136:139], v202, s[20:23], s59 offen nt\n\t"
    "buffer_load_dwordx4 v[140:143], v203, s[20:23], s59 offen nt\n\t"
    "buffer_load_dwordx4 v[144:147], v204, s[20:23], s59 offen nt\n\t"
    "buffer_load_dwordx4 v[148:151], v205, s[20:23], s59 offen nt\n\t"
    "buffer_load_dwordx4 v[152:155], v206, s[20:23], s59 offen nt\n\t"
    "buffer_load_dwordx4 v[156:159], v207, s[20:23], s59 offen nt\n\t"
    "s_add_u32 s48, s44, s84\n\t"
    "s_addc_u32 s49, s45, 0\n\t"
    "s_and_b32 s49, s49, 0xffff\n\t"
    "buffer_load_dword v213, v212, s[48:51], 0 offen sc1\n\t"
    "s_and_b32 s45, s45, 0xffff\n\t"
    "s_and_b32 s41, s41, 0xffff\n\t"
    "v_lshlrev_b32 v240, 11, v224\n\t"
    "v_lshl_add_u32 v240, v225, 4, v240\n\t"
    "s_lshl_b32 s79, s66, 12\n\t"
    "s_lshl_b32 s75, s70, 18\n\t"
    "s_add_u32 s75, s75, s79\n\t"
    "s_add_u32 s75, s75, 65536\n\t"
    "s_add_u32 s79, s79, 98304\n\t"
    "v_add_u32 v211, s79, v240\n\t"
    "s_lshl_b32 s62, s71, 14\n\t"
    "s_add_u32 s62, s62, s75\n\t"
    "s_lshl_b32 s81, s71, 2\n\t"
    "v_lshrrev_b32 v246, 4, v255\n\t"
    "v_and_b32 v247, 15, v255\n\t"
    "v_lshrrev_b32 v244, 1, v247\n\t"
    "v_xor_b32 v245, v244, v246\n\t"
    "v_lshlrev_b32 v245, 4, v245\n\t"
    "v_lshl_add_u32 v245, v247, 7, v245\n\t"
    "s_lshl_b32 s54, s66, 13\n\t"
    "v_add_u32 v232, s54, v245\n\t"
    "v_xor_b32 v233, 64, v232\n\t"
    "v_lshlrev_b32 v234, 11, v246\n\t"
    "v_lshl_add_u32 v234, v247, 4, v234\n\t"
    "v_add_u32 v234, 98304, v234\n\t"
    "s_lshl_b32 s54, s69, 2\n\t"
    "s_add_u32 s54, s54, s66\n\t"
    "s_mul_i32 s54, s54, 458752\n\t"
    "s_lshl_b32 s55, s70, 8\n\t"
    "s_add_u32 s54, s54, s55\n\t"
    "v_mul_u32_u24 v244, 28672, v246\n\t"
    "v_lshl_add_u32 v235, v247, 3, v244\n\t"
    "v_add_u32 v235, s54, v235\n\t"
    "v_mov_b32 v0, 0\n\t"
    "v_mov_b32 v1, 0\n\t"
    "v_mov_b32 v2, 0\n\t"
    "v_mov_b32 v3, 0\n\t"
    "v_mov_b32 v4, 0\n\t"
    "v_mov_b32 v5, 0\n\t"
    "v_mov_b32 v6, 0\n\t"
    "v_mov_b32 v7, 0\n\t"
    "v_mov_b32 v8, 0\n\t"
    "v_mov_b32 v9, 0\n\t"
    "v_mov_b32 v10, 0\n\t"
    "v_mov_b32 v11, 0\n\t"
    "v_mov_b32 v12, 0\n\t"
    "v_mov_b32 v13, 0\n\t"
    "v_mov_b32 v14, 0\n\t"
    "v_mov_b32 v15, 0\n\t"
    "v_mov_b32 v16, 0\n\t"
    "v_mov_b32 v17, 0\n\t"
    "v_mov_b32 v18, 0\n\t"
    "v_mov_b32 v19, 0\n\t"
    "v_mov_b32 v20, 0\n\t"
    "v_mov_b32 v21, 0\n\t"
    "v_mov_b32 v22, 0\n\t"
    "v_mov_b32 v23, 0\n\t"
    "v_mov_b32 v24, 0\n\t"
    "v_mov_b32 v25, 0\n\t"
    "v_mov_b32 v26, 0\n\t"
    "v_mov_b32 v27, 0\n\t"
    "v_mov_b32 v28, 0\n\t"
    "v_mov_b32 v29, 0\n\t"
    "v_mov_b32 v30, 0\n\t"
    "v_mov_b32 v31, 0\n\t"
    "v_mov_b32 v32, 0\n\t"
    "v_mov_b32 v33, 0\n\t"
    "v_mov_b32 v34, 0\n\t"
    "v_mov_b32 v35, 0\n\t"
    "v_mov_b32 v36, 0\n\t"
    "v_mov_b32 v37, 0\n\t"
    "v_mov_b32 v38, 0\n\t"
    "v_mov_b32 v39, 0\n\t"
    "v_mov_b32 v40, 0\n\t"
    "v_mov_b32 v41, 0\n\t"
    "v_mov_b32 v42, 0\n\t"
    "v_mov_b32 v43, 0\n\t"
    "v_mov_b32 v44, 0\n\t"
    "v_mov_b32 v45, 0\n\t"
    "v_mov_b32 v46, 0\n\t"
    "v_mov_b32 v47, 0\n\t"
    "v_mov_b32 v48, 0\n\t"
    "v_mov_b32 v49, 0\n\t"
    "v_mov_b32 v50, 0\n\t"
    "v_mov_b32 v51, 0\n\t"
    "v_mov_b32 v52, 0\n\t"
    "v_mov_b32 v53, 0\n\t"
    "v_mov_b32 v54, 0\n\t"
    "v_mov_b32 v55, 0\n\t"
    "v_mov_b32 v56, 0\n\t"
    "v_mov_b32 v57, 0\n\t"
    "v_mov_b32 v58, 0\n\t"
    "v_mov_b32 v59, 0\n\t"
    "v_mov_b32 v60, 0\n\t"
    "v_mov_b32 v61, 0\n\t"
    "v_mov_b32 v62, 0\n\t"
    "v_mov_b32 v63, 0\n\t"
    "v_mov_b32 v64, 0\n\t"
    "v_mov_b32 v65, 0\n\t"
    "v_mov_b32 v66, 0\n\t"
    "v_mov_b32 v67, 0\n\t"
    "v_mov_b32 v68, 0\n\t"
    "v_mov_b32 v69, 0\n\t"
    "v_mov_b32 v70, 0\n\t"
    "v_mov_b32 v71, 0\n\t"
    "v_mov_b32 v72, 0\n\t"
    "v_mov_b32 v73, 0\n\t"
    "v_mov_b32 v74, 0\n\t"
    "v_mov_b32 v75, 0\n\t"
    "v_mov_b32 v76, 0\n\t"
    "v_mov_b32 v77, 0\n\t"
    "v_mov_b32 v78, 0\n\t"
    "v_mov_b32 v79, 0\n\t"
    "v_mov_b32 v80, 0\n\t"
    "v_mov_b32 v81, 0\n\t"
    "v_mov_b32 v82, 0\n\t"
    "v_mov_b32 v83, 0\n\t"
    "v_mov_b32 v84, 0\n\t"
    "v_mov_b32 v85, 0\n\t"
    "v_mov_b32 v86, 0\n\t"
    "v_mov_b32 v87, 0\n\t"
    "v_mov_b32 v88, 0\n\t"
    "v_mov_b32 v89, 0\n\t"
    "v_mov_b32 v90, 0\n\t"
    "v_mov_b32 v91, 0\n\t"
    "v_mov_b32 v92, 0\n\t"
    "v_mov_b32 v93, 0\n\t"
    "v_mov_b32 v94, 0\n\t"
    "v_mov_b32 v95, 0\n\t"
    "v_mov_b32 v96, 0\n\t"
    "v_mov_b32 v97, 0\n\t"
    "v_mov_b32 v98, 0\n\t"
    "v_mov_b32 v99, 0\n\t"
    "v_mov_b32 v100, 0\n\t"
    "v_mov_b32 v101, 0\n\t"
    "v_mov_b32 v102, 0\n\t"
    "v_mov_b32 v103, 0\n\t"
    "v_mov_b32 v104, 0\n\t"
    "v_mov_b32 v105, 0\n\t"
    "v_mov_b32 v106, 0\n\t"
    "v_mov_b32 v107, 0\n\t"
    "v_mov_b32 v108, 0\n\t"
    "v_mov_b32 v109, 0\n\t"
    "v_mov_b32 v110, 0\n\t"
    "v_mov_b32 v111, 0\n\t"
    "v_mov_b32 v112, 0\n\t"
    "v_mov_b32 v113, 0\n\t"
    "v_mov_b32 v114, 0\n\t"
    "v_mov_b32 v115, 0\n\t"
    "v_mov_b32 v116, 0\n\t"
    "v_mov_b32 v117, 0\n\t"
    "v_mov_b32 v118, 0\n\t"
    "v_mov_b32 v119, 0\n\t"
    "v_mov_b32 v120, 0\n\t"
    "v_mov_b32 v121, 0\n\t"
    "v_mov_b32 v122, 0\n\t"
    "v_mov_b32 v123, 0\n\t"
    "v_mov_b32 v124, 0\n\t"
    "v_mov_b32 v125, 0\n\t"
    "v_mov_b32 v126, 0\n\t"
    "v_mov_b32 v127, 0\n\t"
    "s_mov_b32 s64, 0\n\t"
    "s_mov_b32 s65, 0\n\t"
    "s_mov_b32 s80, 4294901760\n\t"
    "v_mov_b32 v242, 0\n\t"
    "s_barrier\n\t"
    "s_waitcnt vmcnt(9)\n\t"
    "v_lshlrev_b32 v176, 16, v192\n\t"
    "v_lshlrev_b32 v180, 16, v194\n\t"
    "v_and_b32 v177, 0xffff0000, v192\n\t"
    "v_and_b32 v181, 0xffff0000, v194\n\t"
    "v_lshlrev_b32 v178, 16, v193\n\t"
    "v_lshlrev_b32 v182, 16, v195\n\t"
    "v_and_b32 v179, 0xffff0000, v193\n\t"
    "v_and_b32 v183, 0xffff0000, v195\n\t"
    "v_mul_f32 v180, -v180, v176\n\t"
    "v_mul_f32 v181, -v181, v177\n\t"
    "v_mul_f32 v182, -v182, v178\n\t"
    "v_mul_f32 v183, -v183, v179\n\t"
    "v_lshlrev_b32 v184, 16, v196\n\t"
    "v_and_b32 v185, 0xffff0000, v196\n\t"
    "v_lshlrev_b32 v186, 16, v197\n\t"
    "v_and_b32 v187, 0xffff0000, v197\n\t"
    "v_lshlrev_b32 v188, 16, v198\n\t"
    "v_and_b32 v189, 0xffff0000, v198\n\t"
    "v_lshlrev_b32 v190, 16, v199\n\t"
    "v_and_b32 v191, 0xffff0000, v199\n\t"
    "s_waitcnt vmcnt(7)\n\t"
    "v_cvt_f32_i32 v128, v128\n\t"
    "v_cvt_f32_i32 v129, v129\n\t"
    "v_cvt_f32_i32 v130, v130\n\t"
    "v_cvt_f32_i32 v131, v131\n\t"
    "v_cvt_f32_i32 v132, v132\n\t"
    "v_cvt_f32_i32 v133, v133\n\t"
    "v_cvt_f32_i32 v134, v134\n\t"
    "v_cvt_f32_i32 v135, v135\n\t"
    "v_fma_f32 v128, v128, v176, v180\n\t"
    "v_fma_f32 v129, v129, v177, v181\n\t"
    "v_fma_f32 v130, v130, v178, v182\n\t"
    "v_fma_f32 v131, v131, v179, v183\n\t"
    "v_fma_f32 v132, v132, v176, v180\n\t"
    "v_fma_f32 v133, v133, v177, v181\n\t"
    "v_fma_f32 v134, v134, v178, v182\n\t"
    "v_fma_f32 v135, v135, v179, v183\n\t"
    "v_mul_f32 v128, v128, v184\n\t"
    "v_mul_f32 v129, v129, v184\n\t"
    "v_mul_f32 v130, v130, v184\n\t"
    "v_mul_f32 v131, v131, v184\n\t"
    "v_mul_f32 v132, v132, v185\n\t"
    "v_mul_f32 v133, v133, v185\n\t"
    "v_mul_f32 v134, v134, v185\n\t"
    "v_mul_f32 v135, v135, v185\n\t"
    "v_cvt_pk_bf16_f32 v160, v128, v132\n\t"
    "v_cvt_pk_bf16_f32 v164, v129, v133\n\t"
    "v_cvt_pk_bf16_f32 v168, v130, v134\n\t"
    "v_cvt_pk_bf16_f32 v172, v131, v135\n\t"
    "s_waitcnt vmcnt(5)\n\t"
    "v_cvt_f32_i32 v136, v136\n\t"
    "v_cvt_f32_i32 v137, v137\n\t"
    "v_cvt_f32_i32 v138, v138\n\t"
    "v_cvt_f32_i32 v139, v139\n\t"
    "v_cvt_f32_i32 v140, v140\n\t"
    "v_cvt_f32_i32 v141, v141\n\t"
    "v_cvt_f32_i32 v142, v142\n\t"
    "v_cvt_f32_i32 v143, v143\n\t"
    "v_fma_f32 v136, v136, v176, v180\n\t"
    "v_fma_f32 v137, v137, v177, v181\n\t"
    "v_fma_f32 v138, v138, v178, v182\n\t"
    "v_fma_f32 v139, v139, v179, v183\n\t"
    "v_fma_f32 v140, v140, v176, v180\n\t"
    "v_fma_f32 v141, v141, v177, v181\n\t"
    "v_fma_f32 v142, v142, v178, v182\n\t"
    "v_fma_f32 v143, v143, v179, v183\n\t"
    "v_mul_f32 v136, v136, v186\n\t"
    "v_mul_f32 v137, v137, v186\n\t"
    "v_mul_f32 v138, v138, v186\n\t"
    "v_mul_f32 v139, v139, v186\n\t"
    "v_mul_f32 v140, v140, v187\n\t"
    "v_mul_f32 v141, v141, v187\n\t"
    "v_mul_f32 v142, v142, v187\n\t"
    "v_mul_f32 v143, v143, v187\n\t"
    "v_cvt_pk_bf16_f32 v161, v136, v140\n\t"
    "v_cvt_pk_bf16_f32 v165, v137, v141\n\t"
    "v_cvt_pk_bf16_f32 v169, v138, v142\n\t"
    "v_cvt_pk_bf16_f32 v173, v139, v143\n\t"
    "s_waitcnt vmcnt(3)\n\t"
    "v_cvt_f32_i32 v144, v144\n\t"
    "v_cvt_f32_i32 v145, v145\n\t"
    "v_cvt_f32_i32 v146, v146\n\t"
    "v_cvt_f32_i32 v147, v147\n\t"
    "v_cvt_f32_i32 v148, v148\n\t"
    "v_cvt_f32_i32 v149, v149\n\t"
    "v_cvt_f32_i32 v150, v150\n\t"
    "v_cvt_f32_i32 v151, v151\n\t"
    "v_fma_f32 v144, v144, v176, v180\n\t"
    "v_fma_f32 v145, v145, v177, v181\n\t"
    "v_fma_f32 v146, v146, v178, v182\n\t"
    "v_fma_f32 v147, v147, v179, v183\n\t"
    "v_fma_f32 v148, v148, v176, v180\n\t"
    "v_fma_f32 v149, v149, v177, v181\n\t"
    "v_fma_f32 v150, v150, v178, v182\n\t"
    "v_fma_f32 v151, v151, v179, v183\n\t"
    "v_mul_f32 v144, v144, v188\n\t"
    "v_mul_f32 v145, v145, v188\n\t"
    "v_mul_f32 v146, v146, v188\n\t"
    "v_mul_f32 v147, v147, v188\n\t"
    "v_mul_f32 v148, v148, v189\n\t"
    "v_mul_f32 v149, v149, v189\n\t"
    "v_mul_f32 v150, v150, v189\n\t"
    "v_mul_f32 v151, v151, v189\n\t"
    "v_cvt_pk_bf16_f32 v162, v144, v148\n\t"
    "v_cvt_pk_bf16_f32 v166, v145, v149\n\t"
    "v_cvt_pk_bf16_f32 v170, v146, v150\n\t"
    "v_cvt_pk_bf16_f32 v174, v147, v151\n\t"
    "s_waitcnt vmcnt(1)\n\t"
    "v_cvt_f32_i32 v152, v152\n\t"
    "v_cvt_f32_i32 v153, v153\n\t"
    "v_cvt_f32_i32 v154, v154\n\t"
    "v_cvt_f32_i32 v155, v155\n\t"
    "v_cvt_f32_i32 v156, v156\n\t"
    "v_cvt_f32_i32 v157, v157\n\t"
    "v_cvt_f32_i32 v158, v158\n\t"
    "v_cvt_f32_i32 v159, v159\n\t"
    "v_fma_f32 v152, v152, v176, v180\n\t"
    "v_fma_f32 v153, v153, v177, v181\n\t"
    "v_fma_f32 v154, v154, v178, v182\n\t"
    "v_fma_f32 v155, v155, v179, v183\n\t"
    "v_fma_f32 v156, v156, v176, v180\n\t"
    "v_fma_f32 v157, v157, v177, v181\n\t"
    "v_fma_f32 v158, v158, v178, v182\n\t"
    "v_fma_f32 v159, v159, v179, v183\n\t"
    "v_mul_f32 v152, v152, v190\n\t"
    "v_mul_f32 v153, v153, v190\n\t"
    "v_mul_f32 v154, v154, v190\n\t"
    "v_mul_f32 v155, v155, v190\n\t"
    "v_mul_f32 v156, v156, v191\n\t"
    "v_mul_f32 v157, v157, v191\n\t"
    "v_mul_f32 v158, v158, v191\n\t"
    "v_mul_f32 v159, v159, v191\n\t"
    "v_cvt_pk_bf16_f32 v163, v152, v156\n\t"
    "v_cvt_pk_bf16_f32 v167, v153, v157\n\t"
    "v_cvt_pk_bf16_f32 v171, v154, v158\n\t"
    "v_cvt_pk_bf16_f32 v175, v155, v159\n\t"
    "ds_write_b128 v211, v[160:163] offset:0\n\t"
    "ds_write_b128 v211, v[164:167] offset:512\n\t"
    "ds_write_b128 v211, v[168:171] offset:1024\n\t"
    "ds_write_b128 v211, v[172:175] offset:1536\n\t"
    "buffer_store_dwordx4 v[160:163], v240, s[44:47], s62 offen offset:0 sc1\n\t"
    "buffer_store_dwordx4 v[164:167], v240, s[44:47], s62 offen offset:512 sc1\n\t"
    "buffer_store_dwordx4 v[168:171], v240, s[44:47], s62 offen offset:1024 sc1\n\t"
    "buffer_store_dwordx4 v[172:175], v240, s[44:47], s62 offen offset:1536 sc1\n\t"
    "s_waitcnt vmcnt(4)\n\t"
    "v_readlane_b32 s13, v213, s71\n\t"
    "s_setprio 0\n\t"
    "s_nop 2\n\t"
    "s_add_u32 s13, s13, 1\n\t"
    "v_mov_b32 v241, s13\n\t"
    "s_waitcnt lgkmcnt(0)\n\t"
    "s_barrier\n\t"
    "ds_read_b128 v[128:131], v232 offset:0\n\t"
    "ds_read_b128 v[160:163], v234 offset:0\n\t"
    "ds_read_b128 v[132:135], v232 offset:2048\n\t"
    "ds_read_b128 v[136:139], v232 offset:4096\n\t"
    "ds_read_b128 v[140:143], v232 offset:6144\n\t"
    "ds_read_b128 v[164:167], v234 offset:256\n\t"
    "ds_read_b128 v[168:171], v234 offset:512\n\t"
    "ds_read_b128 v[172:175], v234 offset:768\n\t"
    "ds_read_b128 v[176:179], v234 offset:1024\n\t"
    "ds_read_b128 v[180:183], v234 offset:1280\n\t"
    "ds_read_b128 v[184:187], v234 offset:1536\n\t"
    "ds_read_b128 v[188:191], v234 offset:1792\n\t"
    "s_waitcnt lgkmcnt(6)\n\t"
    "v_mfma_f32_16x16x32_bf16 v[0:3], v[128:131], v[160:163], v[0:3]\n\t"
    "ds_read_b128 v[144:147], v233 offset:0\n\t"
    "v_mfma_f32_16x16x32_bf16 v[32:35], v[132:135], v[160:163], v[32:35]\n\t"
    "v_mfma_f32_16x16x32_bf16 v[64:67], v[136:139], v[160:163], v[64:67]\n\t"
    "ds_read_b128 v[192:195], v234 offset:8192\n\t"
    "v_mfma_f32_16x16x32_bf16 v[96:99], v[140:143], v[160:163], v[96:99]\n\t"
    "v_mfma_f32_16x16x32_bf16 v[4:7], v[128:131], v[164:167], v[4:7]\n\t"
    "ds_read_b128 v[148:151], v233 offset:2048\n\t"
    "v_mfma_f32_16x16x32_bf16 v[36:39], v[132:135], v[164:167], v[36:39]\n\t"
    "v_mfma_f32_16x16x32_bf16 v[68:71], v[136:139], v[164:167], v[68:71]\n\t"
    "ds_read_b128 v[152:155], v233 offset:4096\n\t"
    "v_mfma_f32_16x16x32_bf16 v[100:103], v[140:143], v[164:167], v[100:103]\n\t"
    "s_waitcnt lgkmcnt(4)\n\t"
    "v_mfma_f32_16x16x32_bf16 v[8:11], v[128:131], v[168:171], v[8:11]\n\t"
    "ds_read_b128 v[156:159], v233 offset:6144\n\t"
    "v_mfma_f32_16x16x32_bf16 v[40:43], v[132:135], v[168:171], v[40:43]\n\t"
    "v_mfma_f32_16x16x32_bf16 v[72:75], v[136:139], v[168:171], v[72:75]\n\t"
    "ds_read_b128 v[196:199], v234 offset:8448\n\t"
    "v_mfma_f32_16x16x32_bf16 v[104:107], v[140:143], v[168:171], v[104:107]\n\t"
    "v_mfma_f32_16x16x32_bf16 v[12:15], v[128:131], v[172:175], v[12:15]\n\t"
    "ds_read_b128 v[200:203], v234 offset:8704\n\t"
    "v_mfma_f32_16x16x32_bf16 v[44:47], v[132:135], v[172:175], v[44:47]\n\t"
    "v_mfma_f32_16x16x32_bf16 v[76:79], v[136:139], v[172:175], v[76:79]\n\t"
    "ds_read_b128 v[204:207], v234 offset:8960\n\t"
    "v_mfma_f32_16x16x32_bf16 v[108:111], v[140:143], v[172:175], v[108:111]\n\t"
    "v_mfma_f32_16x16x32_bf16 v[16:19], v[128:131], v[176:179], v[16:19]\n\t"
    "ds_read_b128 v[208:211], v234 offset:9216\n\t"
    "v_mfma_f32_16x16x32_bf16 v[48:51], v[132:135], v[176:179], v[48:51]\n\t"
    "v_mfma_f32_16x16x32_bf16 v[80:83], v[136:139], v[176:179], v[80:83]\n\t"
    "ds_read_b128 v[212:215], v234 offset:9472\n\t"
    "v_mfma_f32_16x16x32_bf16 v[112:115], v[140:143], v[176:179], v[112:115]\n\t"
    "v_mfma_f32_16x16x32_bf16 v[20:23], v[128:131], v[180:183], v[20:23]\n\t"
    "ds_read_b128 v[216:219], v234 offset:9728\n\t"
    "v_mfma_f32_16x16x32_bf16 v[52:55], v[132:135], v[180:183], v[52:55]\n\t"
    "v_mfma_f32_16x16x32_bf16 v[84:87], v[136:139], v[180:183], v[84:87]\n\t"
    "ds_read_b128 v[220:223], v234 offset:9984\n\t"
    "v_mfma_f32_16x16x32_bf16 v[116:119], v[140:143], v[180:183], v[116:119]\n\t"
    "s_add_u32 s65, s65, 32768\n\t"
    "v_mfma_f32_16x16x32_bf16 v[24:27], v[128:131], v[184:187], v[24:27]\n\t"
    "s_cmp_eq_u32 s65, 98304\n\t"
    "s_cselect_b32 s54, s80, 32768\n\t"
    "v_mfma_f32_16x16x32_bf16 v[56:59], v[132:135], v[184:187], v[56:59]\n\t"
    "s_cselect_b32 s65, 0, s65\n\t"
    "s_ashr_i32 s55, s54, 1\n\t"
    "v_mfma_f32_16x16x32_bf16 v[88:91], v[136:139], v[184:187], v[88:91]\n\t"
    "v_add_u32 v232, s54, v232\n\t"
    "v_mfma_f32_16x16x32_bf16 v[120:123], v[140:143], v[184:187], v[120:123]\n\t"
    "v_add_u32 v233, s54, v233\n\t"
    "v_mfma_f32_16x16x32_bf16 v[28:31], v[128:131], v[188:191], v[28:31]\n\t"
    "v_add_u32 v234, s55, v234\n\t"
    "v_mfma_f32_16x16x32_bf16 v[60:63], v[132:135], v[188:191], v[60:63]\n\t"
    "v_mfma_f32_16x16x32_bf16 v[92:95], v[136:139], v[188:191], v[92:95]\n\t"
    "v_mfma_f32_16x16x32_bf16 v[124:127], v[140:143], v[188:191], v[124:127]\n\t"
    "s_waitcnt lgkmcnt(0)\n\t"
    "v_mfma_f32_16x16x32_bf16 v[0:3], v[144:147], v[192:195], v[0:3]\n\t"
    "v_mfma_f32_16x16x32_bf16 v[32:35], v[148:151], v[192:195], v[32:35]\n\t"
    "v_mfma_f32_16x16x32_bf16 v[64:67], v[152:155], v[192:195], v[64:67]\n\t"
    "v_mfma_f32_16x16x32_bf16 v[96:99], v[156:159], v[192:195], v[96:99]\n\t"
    "v_mfma_f32_16x16x32_bf16 v[4:7], v[144:147], v[196:199], v[4:7]\n\t"
    "v_mfma_f32_16x16x32_bf16 v[36:39], v[148:151], v[196:199], v[36:39]\n\t"
    "v_mfma_f32_16x16x32_bf16 v[68:71], v[152:155], v[196:199], v[68:71]\n\t"
    "v_mfma_f32_16x16x32_bf16 v[100:103], v[156:159], v[196:199], v[100:103]\n\t"
    "v_mfma_f32_16x16x32_bf16 v[8:11], v[144:147], v[200:203], v[8:11]\n\t"
    "v_mfma_f32_16x16x32_bf16 v[40:43], v[148:151], v[200:203], v[40:43]\n\t"
    "v_mfma_f32_16x16x32_bf16 v[72:75], v[152:155], v[200:203], v[72:75]\n\t"
    "v_mfma_f32_16x16x32_bf16 v[104:107], v[156:159], v[200:203], v[104:107]\n\t"
    "v_mfma_f32_16x16x32_bf16 v[12:15], v[144:147], v[204:207], v[12:15]\n\t"
    "v_mfma_f32_16x16x32_bf16 v[44:47], v[148:151], v[204:207], v[44:47]\n\t"
    "v_mfma_f32_16x16x32_bf16 v[76:79], v[152:155], v[204:207], v[76:79]\n\t"
    "v_mfma_f32_16x16x32_bf16 v[108:111], v[156:159], v[204:207], v[108:111]\n\t"
    "s_waitcnt vmcnt(0)\n\t"
    "s_mov_b64 exec, 1\n\t"
    "buffer_store_dword v241, v242, s[48:51], s81 offen sc1\n\t"
    "s_mov_b64 exec, -1\n\t"
    "s_nop 3\n\t"
    "s_barrier\n\t"
    "v_mfma_f32_16x16x32_bf16 v[16:19], v[144:147], v[208:211], v[16:19]\n\t"
    "ds_read_b128 v[128:131], v232 offset:0\n\t"
    "v_mfma_f32_16x16x32_bf16 v[48:51], v[148:151], v[208:211], v[48:51]\n\t"
    "ds_read_b128 v[160:163], v234 offset:0\n\t"
    "v_mfma_f32_16x16x32_bf16 v[80:83], v[152:155], v[208:211], v[80:83]\n\t"
    "ds_read_b128 v[132:135], v232 offset:2048\n\t"
    "v_mfma_f32_16x16x32_bf16 v[112:115], v[156:159], v[208:211], v[112:115]\n\t"
    "ds_read_b128 v[136:139], v232 offset:4096\n\t"
    "v_mfma_f32_16x16x32_bf16 v[20:23], v[144:147], v[212:215], v[20:23]\n\t"
    "ds_read_b128 v[140:143], v232 offset:6144\n\t"
    "v_mfma_f32_16x16x32_bf16 v[52:55], v[148:151], v[212:215], v[52:55]\n\t"
    "ds_read_b128 v[164:167], v234 offset:256\n\t"
    "v_mfma_f32_16x16x32_bf16 v[84:87], v[152:155], v[212:215], v[84:87]\n\t"
    "ds_read_b128 v[168:171], v234 offset:512\n\t"
    "v_mfma_f32_16x16x32_bf16 v[116:119], v[156:159], v[212:215], v[116:119]\n\t"
    "ds_read_b128 v[172:175], v234 offset:768\n\t"
    "v_mfma_f32_16x16x32_bf16 v[24:27], v[144:147], v[216:219], v[24:27]\n\t"
    "ds_read_b128 v[176:179], v234 offset:1024\n\t"
    "v_mfma_f32_16x16x32_bf16 v[56:59], v[148:151], v[216:219], v[56:59]\n\t"
    "ds_read_b128 v[180:183], v234 offset:1280\n\t"
    "v_mfma_f32_16x16x32_bf16 v[88:91], v[152:155], v[216:219], v[88:91]\n\t"
    "ds_read_b128 v[184:187], v234 offset:1536\n\t"
    "v_mfma_f32_16x16x32_bf16 v[120:123], v[156:159], v[216:219], v[120:123]\n\t"
    "ds_read_b128 v[188:191], v234 offset:1792\n\t"
    "v_mfma_f32_16x16x32_bf16 v[28:31], v[144:147], v[220:223], v[28:31]\n\t"
    "v_mfma_f32_16x16x32_bf16 v[60:63], v[148:151], v[220:223], v[60:63]\n\t"
    "s_add_u32 s64, s64, 1\n\t"
    "v_mfma_f32_16x16x32_bf16 v[92:95], v[152:155], v[220:223], v[92:95]\n\t"
    "v_mfma_f32_16x16x32_bf16 v[124:127], v[156:159], v[220:223], v[124:127]\n\t"
    "Lcloop%=:\n\t"
    "s_waitcnt lgkmcnt(6)\n\t"
    "v_mfma_f32_16x16x32_bf16 v[0:3], v[128:131], v[160:163], v[0:3]\n\t"
    "ds_read_b128 v[144:147], v233 offset:0\n\t"
    "v_mfma_f32_16x16x32_bf16 v[32:35], v[132:135], v[160:163], v[32:35]\n\t"
    "v_mfma_f32_16x16x32_bf16 v[64:67], v[136:139], v[160:163], v[64:67]\n\t"
    "ds_read_b128 v[192:195], v234 offset:8192\n\t"
    "v_mfma_f32_16x16x32_bf16 v[96:99], v[140:143], v[160:163], v[96:99]\n\t"
    "v_mfma_f32_16x16x32_bf16 v[4:7], v[128:131], v[164:167], v[4:7]\n\t"
    "ds_read_b128 v[148:151], v233 offset:2048\n\t"
    "v_mfma_f32_16x16x32_bf16 v[36:39], v[132:135], v[164:167], v[36:39]\n\t"
    "v_mfma_f32_16x16x32_bf16 v[68:71], v[136:139], v[164:167], v[68:71]\n\t"
    "ds_read_b128 v[152:155], v233 offset:4096\n\t"
    "v_mfma_f32_16x16x32_bf16 v[100:103], v[140:143], v[164:167], v[100:103]\n\t"
    "s_waitcnt lgkmcnt(4)\n\t"
    "v_mfma_f32_16x16x32_bf16 v[8:11], v[128:131], v[168:171], v[8:11]\n\t"
    "ds_read_b128 v[156:159], v233 offset:6144\n\t"
    "v_mfma_f32_16x16x32_bf16 v[40:43], v[132:135], v[168:171], v[40:43]\n\t"
    "v_mfma_f32_16x16x32_bf16 v[72:75], v[136:139], v[168:171], v[72:75]\n\t"
    "ds_read_b128 v[196:199], v234 offset:8448\n\t"
    "v_mfma_f32_16x16x32_bf16 v[104:107], v[140:143], v[168:171], v[104:107]\n\t"
    "v_mfma_f32_16x16x32_bf16 v[12:15], v[128:131], v[172:175], v[12:15]\n\t"
    "ds_read_b128 v[200:203], v234 offset:8704\n\t"
    "v_mfma_f32_16x16x32_bf16 v[44:47], v[132:135], v[172:175], v[44:47]\n\t"
    "v_mfma_f32_16x16x32_bf16 v[76:79], v[136:139], v[172:175], v[76:79]\n\t"
    "ds_read_b128 v[204:207], v234 offset:8960\n\t"
    "v_mfma_f32_16x16x32_bf16 v[108:111], v[140:143], v[172:175], v[108:111]\n\t"
    "v_mfma_f32_16x16x32_bf16 v[16:19], v[128:131], v[176:179], v[16:19]\n\t"
    "ds_read_b128 v[208:211], v234 offset:9216\n\t"
    "v_mfma_f32_16x16x32_bf16 v[48:51], v[132:135], v[176:179], v[48:51]\n\t"
    "v_mfma_f32_16x16x32_bf16 v[80:83], v[136:139], v[176:179], v[80:83]\n\t"
    "ds_read_b128 v[212:215], v234 offset:9472\n\t"
    "v_mfma_f32_16x16x32_bf16 v[112:115], v[140:143], v[176:179], v[112:115]\n\t"
    "v_mfma_f32_16x16x32_bf16 v[20:23], v[128:131], v[180:183], v[20:23]\n\t"
    "ds_read_b128 v[216:219], v234 offset:9728\n\t"
    "v_mfma_f32_16x16x32_bf16 v[52:55], v[132:135], v[180:183], v[52:55]\n\t"
    "v_mfma_f32_16x16x32_bf16 v[84:87], v[136:139], v[180:183], v[84:87]\n\t"
    "ds_read_b128 v[220:223], v234 offset:9984\n\t"
    "v_mfma_f32_16x16x32_bf16 v[116:119], v[140:143], v[180:183], v[116:119]\n\t"
    "s_add_u32 s65, s65, 32768\n\t"
    "v_mfma_f32_16x16x32_bf16 v[24:27], v[128:131], v[184:187], v[24:27]\n\t"
    "s_cmp_eq_u32 s65, 98304\n\t"
    "s_cselect_b32 s54, s80, 32768\n\t"
    "v_mfma_f32_16x16x32_bf16 v[56:59], v[132:135], v[184:187], v[56:59]\n\t"
    "s_cselect_b32 s65, 0, s65\n\t"
    "s_ashr_i32 s55, s54, 1\n\t"
    "v_mfma_f32_16x16x32_bf16 v[88:91], v[136:139], v[184:187], v[88:91]\n\t"
    "v_add_u32 v232, s54, v232\n\t"
    "v_mfma_f32_16x16x32_bf16 v[120:123], v[140:143], v[184:187], v[120:123]\n\t"
    "v_add_u32 v233, s54, v233\n\t"
    "v_mfma_f32_16x16x32_bf16 v[28:31], v[128:131], v[188:191], v[28:31]\n\t"
    "v_add_u32 v234, s55, v234\n\t"
    "v_mfma_f32_16x16x32_bf16 v[60:63], v[132:135], v[188:191], v[60:63]\n\t"
    "v_mfma_f32_16x16x32_bf16 v[92:95], v[136:139], v[188:191], v[92:95]\n\t"
    "v_mfma_f32_16x16x32_bf16 v[124:127], v[140:143], v[188:191], v[124:127]\n\t"
    "s_waitcnt lgkmcnt(0)\n\t"
    "v_mfma_f32_16x16x32_bf16 v[0:3], v[144:147], v[192:195], v[0:3]\n\t"
    "v_mfma_f32_16x16x32_bf16 v[32:35], v[148:151], v[192:195], v[32:35]\n\t"
    "v_mfma_f32_16x16x32_bf16 v[64:67], v[152:155], v[192:195], v[64:67]\n\t"
    "v_mfma_f32_16x16x32_bf16 v[96:99], v[156:159], v[192:195], v[96:99]\n\t"
    "v_mfma_f32_16x16x32_bf16 v[4:7], v[144:147], v[196:199], v[4:7]\n\t"
    "v_mfma_f32_16x16x32_bf16 v[36:39], v[148:151], v[196:199], v[36:39]\n\t"
    "v_mfma_f32_16x16x32_bf16 v[68:71], v[152:155], v[196:199], v[68:71]\n\t"
    "v_mfma_f32_16x16x32_bf16 v[100:103], v[156:159], v[196:199], v[100:103]\n\t"
    "v_mfma_f32_16x16x32_bf16 v[8:11], v[144:147], v[200:203], v[8:11]\n\t"
    "v_mfma_f32_16x16x32_bf16 v[40:43], v[148:151], v[200:203], v[40:43]\n\t"
    "v_mfma_f32_16x16x32_bf16 v[72:75], v[152:155], v[200:203], v[72:75]\n\t"
    "v_mfma_f32_16x16x32_bf16 v[104:107], v[156:159], v[200:203], v[104:107]\n\t"
    "v_mfma_f32_16x16x32_bf16 v[12:15], v[144:147], v[204:207], v[12:15]\n\t"
    "v_mfma_f32_16x16x32_bf16 v[44:47], v[148:151], v[204:207], v[44:47]\n\t"
    "v_mfma_f32_16x16x32_bf16 v[76:79], v[152:155], v[204:207], v[76:79]\n\t"
    "v_mfma_f32_16x16x32_bf16 v[108:111], v[156:159], v[204:207], v[108:111]\n\t"
    "s_barrier\n\t"
    "v_mfma_f32_16x16x32_bf16 v[16:19], v[144:147], v[208:211], v[16:19]\n\t"
    "ds_read_b128 v[128:131], v232 offset:0\n\t"
    "v_mfma_f32_16x16x32_bf16 v[48:51], v[148:151], v[208:211], v[48:51]\n\t"
    "ds_read_b128 v[160:163], v234 offset:0\n\t"
    "v_mfma_f32_16x16x32_bf16 v[80:83], v[152:155], v[208:211], v[80:83]\n\t"
    "ds_read_b128 v[132:135], v232 offset:2048\n\t"
    "v_mfma_f32_16x16x32_bf16 v[112:115], v[156:159], v[208:211], v[112:115]\n\t"
    "ds_read_b128 v[136:139], v232 offset:4096\n\t"
    "v_mfma_f32_16x16x32_bf16 v[20:23], v[144:147], v[212:215], v[20:23]\n\t"
    "ds_read_b128 v[140:143], v232 offset:6144\n\t"
    "v_mfma_f32_16x16x32_bf16 v[52:55], v[148:151], v[212:215], v[52:55]\n\t"
    "ds_read_b128 v[164:167], v234 offset:256\n\t"
    "v_mfma_f32_16x16x32_bf16 v[84:87], v[152:155], v[212:215], v[84:87]\n\t"
    "ds_read_b128 v[168:171], v234 offset:512\n\t"
    "v_mfma_f32_16x16x32_bf16 v[116:119], v[156:159], v[212:215], v[116:119]\n\t"
    "ds_read_b128 v[172:175], v234 offset:768\n\t"
    "v_mfma_f32_16x16x32_bf16 v[24:27], v[144:147], v[216:219], v[24:27]\n\t"
    "ds_read_b128 v[176:179], v234 offset:1024\n\t"
    "v_mfma_f32_16x16x32_bf16 v[56:59], v[148:151], v[216:219], v[56:59]\n\t"
    "ds_read_b128 v[180:183], v234 offset:1280\n\t"
    "v_mfma_f32_16x16x32_bf16 v[88:91], v[152:155], v[216:219], v[88:91]\n\t"
    "ds_read_b128 v[184:187], v234 offset:1536\n\t"
    "v_mfma_f32_16x16x32_bf16 v[120:123], v[156:159], v[216:219], v[120:123]\n\t"
    "ds_read_b128 v[188:191], v234 offset:1792\n\t"
    "v_mfma_f32_16x16x32_bf16 v[28:31], v[144:147], v[220:223], v[28:31]\n\t"
    "v_mfma_f32_16x16x32_bf16 v[60:63], v[148:151], v[220:223], v[60:63]\n\t"
    "s_add_u32 s64, s64, 1\n\t"
    "v_mfma_f32_16x16x32_bf16 v[92:95], v[152:155], v[220:223], v[92:95]\n\t"
    "s_cmp_lt_u32 s64, 14\n\t"
    "v_mfma_f32_16x16x32_bf16 v[124:127], v[156:159], v[220:223], v[124:127]\n\t"
    "s_cbranch_scc1 Lcloop%=\n\t"
    "v_add_u32 v236, s80, v232\n\t"
    "v_add_u32 v237, s80, v233\n\t"
    "v_subrev_u32 v238, 32768, v234\n\t"
    "s_waitcnt lgkmcnt(10)\n\t"
    "v_mfma_f32_16x16x32_bf16 v[0:3], v[128:131], v[160:163], v[0:3]\n\t"
    "ds_read_b128 v[144:147], v233 offset:0\n\t"
    "s_waitcnt lgkmcnt(10)\n\t"
    "v_mfma_f32_16x16x32_bf16 v[32:35], v[132:135], v[160:163], v[32:35]\n\t"
    "ds_read_b128 v[192:195], v234 offset:8192\n\t"
    "s_waitcnt lgkmcnt(8)\n\t"
    "v_mfma_f32_16x16x32_bf16 v[4:7], v[128:131], v[164:167], v[4:7]\n\t"
    "ds_read_b128 v[148:151], v233 offset:2048\n\t"
    "v_mfma_f32_16x16x32_bf16 v[36:39], v[132:135], v[164:167], v[36:39]\n\t"
    "ds_read_b128 v[196:199], v234 offset:8448\n\t"
    "s_waitcnt lgkmcnt(9)\n\t"
    "v_mfma_f32_16x16x32_bf16 v[8:11], v[128:131], v[168:171], v[8:11]\n\t"
    "ds_read_b128 v[200:203], v234 offset:8704\n\t"
    "v_mfma_f32_16x16x32_bf16 v[40:43], v[132:135], v[168:171], v[40:43]\n\t"
    "ds_read_b128 v[204:207], v234 offset:8960\n\t"
    "s_waitcnt lgkmcnt(10)\n\t"
    "v_mfma_f32_16x16x32_bf16 v[12:15], v[128:131], v[172:175], v[12:15]\n\t"
    "ds_read_b128 v[208:211], v234 offset:9216\n\t"
    "v_mfma_f32_16x16x32_bf16 v[44:47], v[132:135], v[172:175], v[44:47]\n\t"
    "ds_read_b128 v[212:215], v234 offset:9472\n\t"
    "s_waitcnt lgkmcnt(11)\n\t"
    "v_mfma_f32_16x16x32_bf16 v[16:19], v[128:131], v[176:179], v[16:19]\n\t"
    "ds_read_b128 v[216:219], v234 offset:9728\n\t"
    "v_mfma_f32_16x16x32_bf16 v[48:51], v[132:135], v[176:179], v[48:51]\n\t"
    "ds_read_b128 v[220:223], v234 offset:9984\n\t"
    "s_waitcnt lgkmcnt(12)\n\t"
    "v_mfma_f32_16x16x32_bf16 v[20:23], v[128:131], v[180:183], v[20:23]\n\t"
    "v_mfma_f32_16x16x32_bf16 v[52:55], v[132:135], v[180:183], v[52:55]\n\t"
    "s_waitcnt lgkmcnt(11)\n\t"
    "v_mfma_f32_16x16x32_bf16 v[24:27], v[128:131], v[184:187], v[24:27]\n\t"
    "v_mfma_f32_16x16x32_bf16 v[56:59], v[132:135], v[184:187], v[56:59]\n\t"
    "s_waitcnt lgkmcnt(10)\n\t"
    "v_mfma_f32_16x16x32_bf16 v[28:31], v[128:131], v[188:191], v[28:31]\n\t"
    "v_mfma_f32_16x16x32_bf16 v[60:63], v[132:135], v[188:191], v[60:63]\n\t"
    "s_waitcnt lgkmcnt(0)\n\t"
    "s_barrier\n\t"
    "v_mfma_f32_16x16x32_bf16 v[0:3], v[144:147], v[192:195], v[0:3]\n\t"
    "ds_read_b128 v[128:131], v236 offset:0\n\t"
    "v_mfma_f32_16x16x32_bf16 v[32:35], v[148:151], v[192:195], v[32:35]\n\t"
    "ds_read_b128 v[160:163], v238 offset:0\n\t"
    "v_mfma_f32_16x16x32_bf16 v[4:7], v[144:147], v[196:199], v[4:7]\n\t"
    "ds_read_b128 v[132:135], v236 offset:2048\n\t"
    "v_mfma_f32_16x16x32_bf16 v[36:39], v[148:151], v[196:199], v[36:39]\n\t"
    "ds_read_b128 v[164:167], v238 offset:256\n\t"
    "v_mfma_f32_16x16x32_bf16 v[8:11], v[144:147], v[200:203], v[8:11]\n\t"
    "ds_read_b128 v[168:171], v238 offset:512\n\t"
    "v_mfma_f32_16x16x32_bf16 v[40:43], v[148:151], v[200:203], v[40:43]\n\t"
    "ds_read_b128 v[172:175], v238 offset:768\n\t"
    "v_mfma_f32_16x16x32_bf16 v[12:15], v[144:147], v[204:207], v[12:15]\n\t"
    "ds_read_b128 v[176:179], v238 offset:1024\n\t"
    "v_mfma_f32_16x16x32_bf16 v[44:47], v[148:151], v[204:207], v[44:47]\n\t"
    "ds_read_b128 v[180:183], v238 offset:1280\n\t"
    "v_mfma_f32_16x16x32_bf16 v[16:19], v[144:147], v[208:211], v[16:19]\n\t"
    "ds_read_b128 v[184:187], v238 offset:1536\n\t"
    "v_mfma_f32_16x16x32_bf16 v[48:51], v[148:151], v[208:211], v[48:51]\n\t"
    "ds_read_b128 v[188:191], v238 offset:1792\n\t"
    "v_mfma_f32_16x16x32_bf16 v[20:23], v[144:147], v[212:215], v[20:23]\n\t"
    "v_mfma_f32_16x16x32_bf16 v[52:55], v[148:151], v[212:215], v[52:55]\n\t"
    "v_mfma_f32_16x16x32_bf16 v[24:27], v[144:147], v[216:219], v[24:27]\n\t"
    "v_mfma_f32_16x16x32_bf16 v[56:59], v[148:151], v[216:219], v[56:59]\n\t"
    "v_mfma_f32_16x16x32_bf16 v[28:31], v[144:147], v[220:223], v[28:31]\n\t"
    "v_mfma_f32_16x16x32_bf16 v[60:63], v[148:151], v[220:223], v[60:63]\n\t"
    "s_waitcnt lgkmcnt(8)\n\t"
    "v_mfma_f32_16x16x32_bf16 v[0:3], v[128:131], v[160:163], v[0:3]\n\t"
    "ds_read_b128 v[144:147], v237 offset:0\n\t"
    "s_waitcnt lgkmcnt(8)\n\t"
    "v_mfma_f32_16x16x32_bf16 v[32:35], v[132:135], v[160:163], v[32:35]\n\t"
    "ds_read_b128 v[192:195], v238 offset:8192\n\t"
    "s_waitcnt lgkmcnt(8)\n\t"
    "v_mfma_f32_16x16x32_bf16 v[4:7], v[128:131], v[164:167], v[4:7]\n\t"
    "ds_read_b128 v[148:151], v237 offset:2048\n\t"
    "v_mfma_f32_16x16x32_bf16 v[36:39], v[132:135], v[164:167], v[36:39]\n\t"
    "ds_read_b128 v[196:199], v238 offset:8448\n\t"
    "s_waitcnt lgkmcnt(9)\n\t"
    "v_mfma_f32_16x16x32_bf16 v[8:11], v[128:131], v[168:171], v[8:11]\n\t"
    "ds_read_b128 v[200:203], v238 offset:8704\n\t"
    "v_mfma_f32_16x16x32_bf16 v[40:43], v[132:135], v[168:171], v[40:43]\n\t"
    "ds_read_b128 v[204:207], v238 offset:8960\n\t"
    "s_waitcnt lgkmcnt(10)\n\t"
    "v_mfma_f32_16x16x32_bf16 v[12:15], v[128:131], v[172:175], v[12:15]\n\t"
    "ds_read_b128 v[208:211], v238 offset:9216\n\t"
    "v_mfma_f32_16x16x32_bf16 v[44:47], v[132:135], v[172:175], v[44:47]\n\t"
    "ds_read_b128 v[212:215], v238 offset:9472\n\t"
    "s_waitcnt lgkmcnt(11)\n\t"
    "v_mfma_f32_16x16x32_bf16 v[16:19], v[128:131], v[176:179], v[16:19]\n\t"
    "ds_read_b128 v[216:219], v238 offset:9728\n\t"
    "v_mfma_f32_16x16x32_bf16 v[48:51], v[132:135], v[176:179], v[48:51]\n\t"
    "ds_read_b128 v[220:223], v238 offset:9984\n\t"
    "s_waitcnt lgkmcnt(12)\n\t"
    "v_mfma_f32_16x16x32_bf16 v[20:23], v[128:131], v[180:183], v[20:23]\n\t"
    "v_mfma_f32_16x16x32_bf16 v[52:55], v[132:135], v[180:183], v[52:55]\n\t"
    "s_waitcnt lgkmcnt(11)\n\t"
    "v_mfma_f32_16x16x32_bf16 v[24:27], v[128:131], v[184:187], v[24:27]\n\t"
    "v_mfma_f32_16x16x32_bf16 v[56:59], v[132:135], v[184:187], v[56:59]\n\t"
    "s_waitcnt lgkmcnt(10)\n\t"
    "v_mfma_f32_16x16x32_bf16 v[28:31], v[128:131], v[188:191], v[28:31]\n\t"
    "v_mfma_f32_16x16x32_bf16 v[60:63], v[132:135], v[188:191], v[60:63]\n\t"
    "s_waitcnt lgkmcnt(8)\n\t"
    "v_mfma_f32_16x16x32_bf16 v[0:3], v[144:147], v[192:195], v[0:3]\n\t"
    "ds_read_b128 v[136:139], v232 offset:4096\n\t"
    "s_waitcnt lgkmcnt(8)\n\t"
    "v_mfma_f32_16x16x32_bf16 v[32:35], v[148:151], v[192:195], v[32:35]\n\t"
    "ds_read_b128 v[160:163], v234 offset:0\n\t"
    "s_waitcnt lgkmcnt(8)\n\t"
    "v_mfma_f32_16x16x32_bf16 v[4:7], v[144:147], v[196:199], v[4:7]\n\t"
    "ds_read_b128 v[164:167], v234 offset:256\n\t"
    "v_mfma_f32_16x16x32_bf16 v[36:39], v[148:151], v[196:199], v[36:39]\n\t"
    "ds_read_b128 v[168:171], v234 offset:512\n\t"
    "s_waitcnt lgkmcnt(9)\n\t"
    "v_mfma_f32_16x16x32_bf16 v[8:11], v[144:147], v[200:203], v[8:11]\n\t"
    "ds_read_b128 v[172:175], v234 offset:768\n\t"
    "v_mfma_f32_16x16x32_bf16 v[40:43], v[148:151], v[200:203], v[40:43]\n\t"
    "ds_read_b128 v[176:179], v234 offset:1024\n\t"
    "s_waitcnt lgkmcnt(10)\n\t"
    "v_mfma_f32_16x16x32_bf16 v[12:15], v[144:147], v[204:207], v[12:15]\n\t"
    "ds_read_b128 v[180:183], v234 offset:1280\n\t"
    "v_mfma_f32_16x16x32_bf16 v[44:47], v[148:151], v[204:207], v[44:47]\n\t"
    "ds_read_b128 v[184:187], v234 offset:1536\n\t"
    "s_waitcnt lgkmcnt(11)\n\t"
    "v_mfma_f32_16x16x32_bf16 v[16:19], v[144:147], v[208:211], v[16:19]\n\t"
    "ds_read_b128 v[188:191], v234 offset:1792\n\t"
    "v_mfma_f32_16x16x32_bf16 v[48:51], v[148:151], v[208:211], v[48:51]\n\t"
    "s_waitcnt lgkmcnt(11)\n\t"
    "v_mfma_f32_16x16x32_bf16 v[20:23], v[144:147], v[212:215], v[20:23]\n\t"
    "v_mfma_f32_16x16x32_bf16 v[52:55], v[148:151], v[212:215], v[52:55]\n\t"
    "s_waitcnt lgkmcnt(10)\n\t"
    "v_mfma_f32_16x16x32_bf16 v[24:27], v[144:147], v[216:219], v[24:27]\n\t"
    "v_mfma_f32_16x16x32_bf16 v[56:59], v[148:151], v[216:219], v[56:59]\n\t"
    "s_waitcnt lgkmcnt(9)\n\t"
    "v_mfma_f32_16x16x32_bf16 v[28:31], v[144:147], v[220:223], v[28:31]\n\t"
    "v_mfma_f32_16x16x32_bf16 v[60:63], v[148:151], v[220:223], v[60:63]\n\t"
    "s_waitcnt lgkmcnt(7)\n\t"
    "v_mfma_f32_16x16x32_bf16 v[64:67], v[136:139], v[160:163], v[64:67]\n\t"
    "ds_read_b128 v[152:155], v233 offset:4096\n\t"
    "ds_read_b128 v[192:195], v234 offset:8192\n\t"
    "s_waitcnt lgkmcnt(8)\n\t"
    "v_mfma_f32_16x16x32_bf16 v[68:71], v[136:139], v[164:167], v[68:71]\n\t"
    "ds_read_b128 v[196:199], v234 offset:8448\n\t"
    "s_waitcnt lgkmcnt(8)\n\t"
    "v_mfma_f32_16x16x32_bf16 v[72:75], v[136:139], v[168:171], v[72:75]\n\t"
    "ds_read_b128 v[200:203], v234 offset:8704\n\t"
    "s_waitcnt lgkmcnt(8)\n\t"
    "v_mfma_f32_16x16x32_bf16 v[76:79], v[136:139], v[172:175], v[76:79]\n\t"
    "ds_read_b128 v[204:207], v234 offset:8960\n\t"
    "s_waitcnt lgkmcnt(8)\n\t"
    "v_mfma_f32_16x16x32_bf16 v[80:83], v[136:139], v[176:179], v[80:83]\n\t"
    "ds_read_b128 v[208:211], v234 offset:9216\n\t"
    "v_cvt_pk_bf16_f32 v224, v0, v8\n\t"
    "v_cvt_pk_bf16_f32 v225, v16, v24\n\t"
    "s_mov_b32 s56, 0\n\t"
    "buffer_store_dwordx2 v[224:225], v235, s[40:43], s56 offen sc1\n\t"
    "s_waitcnt lgkmcnt(8)\n\t"
    "v_mfma_f32_16x16x32_bf16 v[84:87], v[136:139], v[180:183], v[84:87]\n\t"
    "ds_read_b128 v[212:215], v234 offset:9472\n\t"
    "v_cvt_pk_bf16_f32 v226, v4, v12\n\t"
    "v_cvt_pk_bf16_f32 v227, v20, v28\n\t"
    "s_mov_b32 s56, 128\n\t"
    "buffer_store_dwordx2 v[226:227], v235, s[40:43], s56 offen sc1\n\t"
    "s_waitcnt lgkmcnt(8)\n\t"
    "v_mfma_f32_16x16x32_bf16 v[88:91], v[136:139], v[184:187], v[88:91]\n\t"
    "ds_read_b128 v[216:219], v234 offset:9728\n\t"
    "v_cvt_pk_bf16_f32 v228, v1, v9\n\t"
    "v_cvt_pk_bf16_f32 v229, v17, v25\n\t"
    "s_mov_b32 s56, 7168\n\t"
    "buffer_store_dwordx2 v[228:229], v235, s[40:43], s56 offen sc1\n\t"
    "s_waitcnt lgkmcnt(8)\n\t"
    "v_mfma_f32_16x16x32_bf16 v[92:95], v[136:139], v[188:191], v[92:95]\n\t"
    "ds_read_b128 v[220:223], v234 offset:9984\n\t"
    "v_cvt_pk_bf16_f32 v230, v5, v13\n\t"
    "v_cvt_pk_bf16_f32 v231, v21, v29\n\t"
    "s_mov_b32 s56, 7296\n\t"
    "buffer_store_dwordx2 v[230:231], v235, s[40:43], s56 offen sc1\n\t"
    "s_waitcnt lgkmcnt(7)\n\t"
    "v_mfma_f32_16x16x32_bf16 v[64:67], v[152:155], v[192:195], v[64:67]\n\t"
    "ds_read_b128 v[136:139], v236 offset:4096\n\t"
    "ds_read_b128 v[160:163], v238 offset:0\n\t"
    "v_cvt_pk_bf16_f32 v244, v2, v10\n\t"
    "v_cvt_pk_bf16_f32 v245, v18, v26\n\t"
    "s_mov_b32 s56, 14336\n\t"
    "buffer_store_dwordx2 v[244:245], v235, s[40:43], s56 offen sc1\n\t"
    "s_waitcnt lgkmcnt(8)\n\t"
    "v_mfma_f32_16x16x32_bf16 v[68:71], v[152:155], v[196:199], v[68:71]\n\t"
    "ds_read_b128 v[164:167], v238 offset:256\n\t"
    "v_cvt_pk_bf16_f32 v246, v6, v14\n\t"
    "v_cvt_pk_bf16_f32 v247, v22, v30\n\t"
    "s_mov_b32 s56, 14464\n\t"
    "buffer_store_dwordx2 v[246:247], v235, s[40:43], s56 offen sc1\n\t"
    "s_waitcnt lgkmcnt(8)\n\t"
    "v_mfma_f32_16x16x32_bf16 v[72:75], v[152:155], v[200:203], v[72:75]\n\t"
    "ds_read_b128 v[168:171], v238 offset:512\n\t"
    "v_cvt_pk_bf16_f32 v224, v3, v11\n\t"
    "v_cvt_pk_bf16_f32 v225, v19, v27\n\t"
    "s_mov_b32 s56, 21504\n\t"
    "buffer_store_dwordx2 v[224:225], v235, s[40:43], s56 offen sc1\n\t"
    "s_waitcnt lgkmcnt(8)\n\t"
    "v_mfma_f32_16x16x32_bf16 v[76:79], v[152:155], v[204:207], v[76:79]\n\t"
    "ds_read_b128 v[172:175], v238 offset:768\n\t"
    "v_cvt_pk_bf16_f32 v226, v7, v15\n\t"
    "v_cvt_pk_bf16_f32 v227, v23, v31\n\t"
    "s_mov_b32 s56, 21632\n\t"
    "buffer_store_dwordx2 v[226:227], v235, s[40:43], s56 offen sc1\n\t"
    "s_waitcnt lgkmcnt(8)\n\t"
    "v_mfma_f32_16x16x32_bf16 v[80:83], v[152:155], v[208:211], v[80:83]\n\t"
    "ds_read_b128 v[176:179], v238 offset:1024\n\t"
    "v_cvt_pk_bf16_f32 v228, v32, v40\n\t"
    "v_cvt_pk_bf16_f32 v229, v48, v56\n\t"
    "s_mov_b32 s56, 114688\n\t"
    "buffer_store_dwordx2 v[228:229], v235, s[40:43], s56 offen sc1\n\t"
    "s_waitcnt lgkmcnt(8)\n\t"
    "v_mfma_f32_16x16x32_bf16 v[84:87], v[152:155], v[212:215], v[84:87]\n\t"
    "ds_read_b128 v[180:183], v238 offset:1280\n\t"
    "v_cvt_pk_bf16_f32 v230, v36, v44\n\t"
    "v_cvt_pk_bf16_f32 v231, v52, v60\n\t"
    "s_mov_b32 s56, 114816\n\t"
    "buffer_store_dwordx2 v[230:231], v235, s[40:43], s56 offen sc1\n\t"
    "s_waitcnt lgkmcnt(8)\n\t"
    "v_mfma_f32_16x16x32_bf16 v[88:91], v[152:155], v[216:219], v[88:91]\n\t"
    "ds_read_b128 v[184:187], v238 offset:1536\n\t"
    "v_cvt_pk_bf16_f32 v244, v33, v41\n\t"
    "v_cvt_pk_bf16_f32 v245, v49, v57\n\t"
    "s_mov_b32 s56, 121856\n\t"
    "buffer_store_dwordx2 v[244:245], v235, s[40:43], s56 offen sc1\n\t"
    "s_waitcnt lgkmcnt(8)\n\t"
    "v_mfma_f32_16x16x32_bf16 v[92:95], v[152:155], v[220:223], v[92:95]\n\t"
    "ds_read_b128 v[188:191], v238 offset:1792\n\t"
    "v_cvt_pk_bf16_f32 v246, v37, v45\n\t"
    "v_cvt_pk_bf16_f32 v247, v53, v61\n\t"
    "s_mov_b32 s56, 121984\n\t"
    "buffer_store_dwordx2 v[246:247], v235, s[40:43], s56 offen sc1\n\t"
    "s_waitcnt lgkmcnt(7)\n\t"
    "v_mfma_f32_16x16x32_bf16 v[64:67], v[136:139], v[160:163], v[64:67]\n\t"
    "ds_read_b128 v[152:155], v237 offset:4096\n\t"
    "ds_read_b128 v[192:195], v238 offset:8192\n\t"
    "v_cvt_pk_bf16_f32 v224, v34, v42\n\t"
    "v_cvt_pk_bf16_f32 v225, v50, v58\n\t"
    "s_mov_b32 s56, 129024\n\t"
    "buffer_store_dwordx2 v[224:225], v235, s[40:43], s56 offen sc1\n\t"
    "s_waitcnt lgkmcnt(8)\n\t"
    "v_mfma_f32_16x16x32_bf16 v[68:71], v[136:139], v[164:167], v[68:71]\n\t"
    "ds_read_b128 v[196:199], v238 offset:8448\n\t"
    "v_cvt_pk_bf16_f32 v226, v38, v46\n\t"
    "v_cvt_pk_bf16_f32 v227, v54, v62\n\t"
    "s_mov_b32 s56, 129152\n\t"
    "buffer_store_dwordx2 v[226:227], v235, s[40:43], s56 offen sc1\n\t"
    "s_waitcnt lgkmcnt(8)\n\t"
    "v_mfma_f32_16x16x32_bf16 v[72:75], v[136:139], v[168:171], v[72:75]\n\t"
    "ds_read_b128 v[200:203], v238 offset:8704\n\t"
    "v_cvt_pk_bf16_f32 v228, v35, v43\n\t"
    "v_cvt_pk_bf16_f32 v229, v51, v59\n\t"
    "s_mov_b32 s56, 136192\n\t"
    "buffer_store_dwordx2 v[228:229], v235, s[40:43], s56 offen sc1\n\t"
    "s_waitcnt lgkmcnt(8)\n\t"
    "v_mfma_f32_16x16x32_bf16 v[76:79], v[136:139], v[172:175], v[76:79]\n\t"
    "ds_read_b128 v[204:207], v238 offset:8960\n\t"
    "v_cvt_pk_bf16_f32 v230, v39, v47\n\t"
    "v_cvt_pk_bf16_f32 v231, v55, v63\n\t"
    "s_mov_b32 s56, 136320\n\t"
    "buffer_store_dwordx2 v[230:231], v235, s[40:43], s56 offen sc1\n\t"
    "s_waitcnt lgkmcnt(8)\n\t"
    "v_mfma_f32_16x16x32_bf16 v[80:83], v[136:139], v[176:179], v[80:83]\n\t"
    "ds_read_b128 v[208:211], v238 offset:9216\n\t"
    "s_waitcnt lgkmcnt(8)\n\t"
    "v_mfma_f32_16x16x32_bf16 v[84:87], v[136:139], v[180:183], v[84:87]\n\t"
    "ds_read_b128 v[212:215], v238 offset:9472\n\t"
    "s_waitcnt lgkmcnt(8)\n\t"
    "v_mfma_f32_16x16x32_bf16 v[88:91], v[136:139], v[184:187], v[88:91]\n\t"
    "ds_read_b128 v[216:219], v238 offset:9728\n\t"
    "s_waitcnt lgkmcnt(8)\n\t"
    "v_mfma_f32_16x16x32_bf16 v[92:95], v[136:139], v[188:191], v[92:95]\n\t"
    "ds_read_b128 v[220:223], v238 offset:9984\n\t"
    "s_waitcnt lgkmcnt(7)\n\t"
    "v_mfma_f32_16x16x32_bf16 v[64:67], v[152:155], v[192:195], v[64:67]\n\t"
    "ds_read_b128 v[140:143], v232 offset:6144\n\t"
    "ds_read_b128 v[160:163], v234 offset:0\n\t"
    "s_waitcnt lgkmcnt(8)\n\t"
    "v_mfma_f32_16x16x32_bf16 v[68:71], v[152:155], v[196:199], v[68:71]\n\t"
    "ds_read_b128 v[164:167], v234 offset:256\n\t"
    "s_waitcnt lgkmcnt(8)\n\t"
    "v_mfma_f32_16x16x32_bf16 v[72:75], v[152:155], v[200:203], v[72:75]\n\t"
    "ds_read_b128 v[168:171], v234 offset:512\n\t"
    "s_waitcnt lgkmcnt(8)\n\t"
    "v_mfma_f32_16x16x32_bf16 v[76:79], v[152:155], v[204:207], v[76:79]\n\t"
    "ds_read_b128 v[172:175], v234 offset:768\n\t"
    "s_waitcnt lgkmcnt(8)\n\t"
    "v_mfma_f32_16x16x32_bf16 v[80:83], v[152:155], v[208:211], v[80:83]\n\t"
    "ds_read_b128 v[176:179], v234 offset:1024\n\t"
    "s_waitcnt lgkmcnt(8)\n\t"
    "v_mfma_f32_16x16x32_bf16 v[84:87], v[152:155], v[212:215], v[84:87]\n\t"
    "ds_read_b128 v[180:183], v234 offset:1280\n\t"
    "s_waitcnt lgkmcnt(8)\n\t"
    "v_mfma_f32_16x16x32_bf16 v[88:91], v[152:155], v[216:219], v[88:91]\n\t"
    "ds_read_b128 v[184:187], v234 offset:1536\n\t"
    "s_waitcnt lgkmcnt(8)\n\t"
    "v_mfma_f32_16x16x32_bf16 v[92:95], v[152:155], v[220:223], v[92:95]\n\t"
    "ds_read_b128 v[188:191], v234 offset:1792\n\t"
    "s_waitcnt lgkmcnt(7)\n\t"
    "v_mfma_f32_16x16x32_bf16 v[96:99], v[140:143], v[160:163], v[96:99]\n\t"
    "ds_read_b128 v[156:159], v233 offset:6144\n\t"
    "ds_read_b128 v[192:195], v234 offset:8192\n\t"
    "s_waitcnt lgkmcnt(8)\n\t"
    "v_mfma_f32_16x16x32_bf16 v[100:103], v[140:143], v[164:167], v[100:103]\n\t"
    "ds_read_b128 v[196:199], v234 offset:8448\n\t"
    "s_waitcnt lgkmcnt(8)\n\t"
    "v_mfma_f32_16x16x32_bf16 v[104:107], v[140:143], v[168:171], v[104:107]\n\t"
    "ds_read_b128 v[200:203], v234 offset:8704\n\t"
    "s_waitcnt lgkmcnt(8)\n\t"
    "v_mfma_f32_16x16x32_bf16 v[108:111], v[140:143], v[172:175], v[108:111]\n\t"
    "ds_read_b128 v[204:207], v234 offset:8960\n\t"
    "s_waitcnt lgkmcnt(8)\n\t"
    "v_mfma_f32_16x16x32_bf16 v[112:115], v[140:143], v[176:179], v[112:115]\n\t"
    "ds_read_b128 v[208:211], v234 offset:9216\n\t"
    "v_cvt_pk_bf16_f32 v244, v64, v72\n\t"
    "v_cvt_pk_bf16_f32 v245, v80, v88\n\t"
    "s_mov_b32 s56, 229376\n\t"
    "buffer_store_dwordx2 v[244:245], v235, s[40:43], s56 offen sc1\n\t"
    "s_waitcnt lgkmcnt(8)\n\t"
    "v_mfma_f32_16x16x32_bf16 v[116:119], v[140:143], v[180:183], v[116:119]\n\t"
    "ds_read_b128 v[212:215], v234 offset:9472\n\t"
    "v_cvt_pk_bf16_f32 v246, v68, v76\n\t"
    "v_cvt_pk_bf16_f32 v247, v84, v92\n\t"
    "s_mov_b32 s56, 229504\n\t"
    "buffer_store_dwordx2 v[246:247], v235, s[40:43], s56 offen sc1\n\t"
    "s_waitcnt lgkmcnt(8)\n\t"
    "v_mfma_f32_16x16x32_bf16 v[120:123], v[140:143], v[184:187], v[120:123]\n\t"
    "ds_read_b128 v[216:219], v234 offset:9728\n\t"
    "v_cvt_pk_bf16_f32 v224, v65, v73\n\t"
    "v_cvt_pk_bf16_f32 v225, v81, v89\n\t"
    "s_mov_b32 s56, 236544\n\t"
    "buffer_store_dwordx2 v[224:225], v235, s[40:43], s56 offen sc1\n\t"
    "s_waitcnt lgkmcnt(8)\n\t"
    "v_mfma_f32_16x16x32_bf16 v[124:127], v[140:143], v[188:191], v[124:127]\n\t"
    "ds_read_b128 v[220:223], v234 offset:9984\n\t"
    "v_cvt_pk_bf16_f32 v226, v69, v77\n\t"
    "v_cvt_pk_bf16_f32 v227, v85, v93\n\t"
    "s_mov_b32 s56, 236672\n\t"
    "buffer_store_dwordx2 v[226:227], v235, s[40:43], s56 offen sc1\n\t"
    "s_waitcnt lgkmcnt(7)\n\t"
    "v_mfma_f32_16x16x32_bf16 v[96:99], v[156:159], v[192:195], v[96:99]\n\t"
    "ds_read_b128 v[140:143], v236 offset:6144\n\t"
    "ds_read_b128 v[160:163], v238 offset:0\n\t"
    "v_cvt_pk_bf16_f32 v228, v66, v74\n\t"
    "v_cvt_pk_bf16_f32 v229, v82, v90\n\t"
    "s_mov_b32 s56, 243712\n\t"
    "buffer_store_dwordx2 v[228:229], v235, s[40:43], s56 offen sc1\n\t"
    "s_waitcnt lgkmcnt(8)\n\t"
    "v_mfma_f32_16x16x32_bf16 v[100:103], v[156:159], v[196:199], v[100:103]\n\t"
    "ds_read_b128 v[164:167], v238 offset:256\n\t"
    "v_cvt_pk_bf16_f32 v230, v70, v78\n\t"
    "v_cvt_pk_bf16_f32 v231, v86, v94\n\t"
    "s_mov_b32 s56, 243840\n\t"
    "buffer_store_dwordx2 v[230:231], v235, s[40:43], s56 offen sc1\n\t"
    "s_waitcnt lgkmcnt(8)\n\t"
    "v_mfma_f32_16x16x32_bf16 v[104:107], v[156:159], v[200:203], v[104:107]\n\t"
    "ds_read_b128 v[168:171], v238 offset:512\n\t"
    "v_cvt_pk_bf16_f32 v244, v67, v75\n\t"
    "v_cvt_pk_bf16_f32 v245, v83, v91\n\t"
    "s_mov_b32 s56, 250880\n\t"
    "buffer_store_dwordx2 v[244:245], v235, s[40:43], s56 offen sc1\n\t"
    "s_waitcnt lgkmcnt(8)\n\t"
    "v_mfma_f32_16x16x32_bf16 v[108:111], v[156:159], v[204:207], v[108:111]\n\t"
    "ds_read_b128 v[172:175], v238 offset:768\n\t"
    "v_cvt_pk_bf16_f32 v246, v71, v79\n\t"
    "v_cvt_pk_bf16_f32 v247, v87, v95\n\t"
    "s_mov_b32 s56, 251008\n\t"
    "buffer_store_dwordx2 v[246:247], v235, s[40:43], s56 offen sc1\n\t"
    "s_waitcnt lgkmcnt(8)\n\t"
    "v_mfma_f32_16x16x32_bf16 v[112:115], v[156:159], v[208:211], v[112:115]\n\t"
    "ds_read_b128 v[176:179], v238 offset:1024\n\t"
    "s_waitcnt lgkmcnt(8)\n\t"
    "v_mfma_f32_16x16x32_bf16 v[116:119], v[156:159], v[212:215], v[116:119]\n\t"
    "ds_read_b128 v[180:183], v238 offset:1280\n\t"
    "s_waitcnt lgkmcnt(8)\n\t"
    "v_mfma_f32_16x16x32_bf16 v[120:123], v[156:159], v[216:219], v[120:123]\n\t"
    "ds_read_b128 v[184:187], v238 offset:1536\n\t"
    "s_waitcnt lgkmcnt(8)\n\t"
    "v_mfma_f32_16x16x32_bf16 v[124:127], v[156:159], v[220:223], v[124:127]\n\t"
    "ds_read_b128 v[188:191], v238 offset:1792\n\t"
    "s_waitcnt lgkmcnt(7)\n\t"
    "v_mfma_f32_16x16x32_bf16 v[96:99], v[140:143], v[160:163], v[96:99]\n\t"
    "ds_read_b128 v[156:159], v237 offset:6144\n\t"
    "ds_read_b128 v[192:195], v238 offset:8192\n\t"
    "s_waitcnt lgkmcnt(8)\n\t"
    "v_mfma_f32_16x16x32_bf16 v[100:103], v[140:143], v[164:167], v[100:103]\n\t"
    "ds_read_b128 v[196:199], v238 offset:8448\n\t"
    "s_waitcnt lgkmcnt(8)\n\t"
    "v_mfma_f32_16x16x32_bf16 v[104:107], v[140:143], v[168:171], v[104:107]\n\t"
    "ds_read_b128 v[200:203], v238 offset:8704\n\t"
    "s_waitcnt lgkmcnt(8)\n\t"
    "v_mfma_f32_16x16x32_bf16 v[108:111], v[140:143], v[172:175], v[108:111]\n\t"
    "ds_read_b128 v[204:207], v238 offset:8960\n\t"
    "s_waitcnt lgkmcnt(8)\n\t"
    "v_mfma_f32_16x16x32_bf16 v[112:115], v[140:143], v[176:179], v[112:115]\n\t"
    "ds_read_b128 v[208:211], v238 offset:9216\n\t"
    "s_waitcnt lgkmcnt(8)\n\t"
    "v_mfma_f32_16x16x32_bf16 v[116:119], v[140:143], v[180:183], v[116:119]\n\t"
    "ds_read_b128 v[212:215], v238 offset:9472\n\t"
    "s_waitcnt lgkmcnt(8)\n\t"
    "v_mfma_f32_16x16x32_bf16 v[120:123], v[140:143], v[184:187], v[120:123]\n\t"
    "ds_read_b128 v[216:219], v238 offset:9728\n\t"
    "s_waitcnt lgkmcnt(8)\n\t"
    "v_mfma_f32_16x16x32_bf16 v[124:127], v[140:143], v[188:191], v[124:127]\n\t"
    "ds_read_b128 v[220:223], v238 offset:9984\n\t"
    "s_waitcnt lgkmcnt(7)\n\t"
    "v_mfma_f32_16x16x32_bf16 v[96:99], v[156:159], v[192:195], v[96:99]\n\t"
    "s_waitcnt lgkmcnt(6)\n\t"
    "v_mfma_f32_16x16x32_bf16 v[100:103], v[156:159], v[196:199], v[100:103]\n\t"
    "s_waitcnt lgkmcnt(5)\n\t"
    "v_mfma_f32_16x16x32_bf16 v[104:107], v[156:159], v[200:203], v[104:107]\n\t"
    "s_waitcnt lgkmcnt(4)\n\t"
    "v_mfma_f32_16x16x32_bf16 v[108:111], v[156:159], v[204:207], v[108:111]\n\t"
    "s_waitcnt lgkmcnt(3)\n\t"
    "v_mfma_f32_16x16x32_bf16 v[112:115], v[156:159], v[208:211], v[112:115]\n\t"
    "s_waitcnt lgkmcnt(2)\n\t"
    "v_mfma_f32_16x16x32_bf16 v[116:119], v[156:159], v[212:215], v[116:119]\n\t"
    "s_waitcnt lgkmcnt(1)\n\t"
    "v_mfma_f32_16x16x32_bf16 v[120:123], v[156:159], v[216:219], v[120:123]\n\t"
    "s_waitcnt lgkmcnt(0)\n\t"
    "v_mfma_f32_16x16x32_bf16 v[124:127], v[156:159], v[220:223], v[124:127]\n\t"
    "s_nop 7\n\t"
    "v_cvt_pk_bf16_f32 v224, v96, v104\n\t"
    "v_cvt_pk_bf16_f32 v225, v112, v120\n\t"
    "s_mov_b32 s56, 344064\n\t"
    "buffer_store_dwordx2 v[224:225], v235, s[40:43], s56 offen sc1\n\t"
    "v_cvt_pk_bf16_f32 v226, v100, v108\n\t"
    "v_cvt_pk_bf16_f32 v227, v116, v124\n\t"
    "s_mov_b32 s56, 344192\n\t"
    "buffer_store_dwordx2 v[226:227], v235, s[40:43], s56 offen sc1\n\t"
    "v_cvt_pk_bf16_f32 v228, v97, v105\n\t"
    "v_cvt_pk_bf16_f32 v229, v113, v121\n\t"
    "s_mov_b32 s56, 351232\n\t"
    "buffer_store_dwordx2 v[228:229], v235, s[40:43], s56 offen sc1\n\t"
    "v_cvt_pk_bf16_f32 v230, v101, v109\n\t"
    "v_cvt_pk_bf16_f32 v231, v117, v125\n\t"
    "s_mov_b32 s56, 351360\n\t"
    "buffer_store_dwordx2 v[230:231], v235, s[40:43], s56 offen sc1\n\t"
    "v_cvt_pk_bf16_f32 v244, v98, v106\n\t"
    "v_cvt_pk_bf16_f32 v245, v114, v122\n\t"
    "s_mov_b32 s56, 358400\n\t"
    "buffer_store_dwordx2 v[244:245], v235, s[40:43], s56 offen sc1\n\t"
    "v_cvt_pk_bf16_f32 v246, v102, v110\n\t"
    "v_cvt_pk_bf16_f32 v247, v118, v126\n\t"
    "s_mov_b32 s56, 358528\n\t"
    "buffer_store_dwordx2 v[246:247], v235, s[40:43], s56 offen sc1\n\t"
    "v_cvt_pk_bf16_f32 v224, v99, v107\n\t"
    "v_cvt_pk_bf16_f32 v225, v115, v123\n\t"
    "s_mov_b32 s56, 365568\n\t"
    "buffer_store_dwordx2 v[224:225], v235, s[40:43], s56 offen sc1\n\t"
    "v_cvt_pk_bf16_f32 v226, v103, v111\n\t"
    "v_cvt_pk_bf16_f32 v227, v119, v127\n\t"
    "s_mov_b32 s56, 365696\n\t"
    "buffer_store_dwordx2 v[226:227], v235, s[40:43], s56 offen sc1\n\t"
    "s_branch Lend%=\n\t"
    "Lprod%=:\n\t"
    "s_and_b32 s15, s69, 3\n\t"
    "s_lshl_b32 s12, s15, 2\n\t"
    "s_lshr_b32 s12, 0x64206420, s12\n\t"
    "s_and_b32 s12, s12, 0xffff\n\t"
    "s_lshr_b32 s13, s69, 2\n\t"
    "s_mul_i32 s13, s13, 0x8888\n\t"
    "s_add_u32 s13, s13, s12\n\t"
    "s_xor_b32 s14, s13, 0x8888\n\t"
    "s_lshl_b32 s14, s14, 12\n\t"
    "s_lshr_b32 s12, s13, 4\n\t"
    "s_or_b32 s12, s12, s14\n\t"
    "s_and_b32 s52, s13, 15\n\t"
    "s_mov_b32 s13, 0\n\t"
    "s_lshl_b64 s[12:13], s[12:13], 12\n\t"
    "s_or_b32 s52, s52, s12\n\t"
    "s_bitcmp1_b32 s15, 0\n\t"
    "s_mov_b32 s14, 3751499520\n\t"
    "s_cselect_b32 s14, s14, 4256789760\n\t"
    "s_mov_b32 s12, 304\n\t"
    "s_cselect_b32 s12, s12, 784\n\t"
    "s_or_b32 s53, s13, s14\n\t"
    "s_or_b32 s52, s52, s12\n\t"
    "v_lshrrev_b32 v224, 5, v255\n\t"
    "v_and_b32 v225, 31, v255\n\t"
    "s_lshl_b32 s54, s66, 4\n\t"
    "v_lshl_add_u32 v226, v224, 3, s54\n\t"
    "v_lshlrev_b32 v209, 1, v226\n\t"
    "v_mul_u32_u24 v227, 14336, v226\n\t"
    "v_lshl_add_u32 v200, v225, 4, v227\n\t"
    "v_add_u32 v201, 14336, v200\n\t"
    "v_add_u32 v202, 28672, v200\n\t"
    "v_add_u32 v203, 43008, v200\n\t"
    "v_add_u32 v204, 57344, v200\n\t"
    "v_add_u32 v205, 71680, v200\n\t"
    "v_add_u32 v206, 86016, v200\n\t"
    "v_add_u32 v207, 100352, v200\n\t"
    "v_lshlrev_b32 v208, 3, v225\n\t"
    "s_lshl_b32 s73, s70, 9\n\t"
    "s_lshr_b32 s54, s66, 1\n\t"
    "s_mul_i32 s54, s54, 7168\n\t"
    "s_lshl_b32 s74, s70, 8\n\t"
    "s_add_u32 s74, s74, s54\n\t"
    "v_lshlrev_b32 v240, 11, v224\n\t"
    "v_lshl_add_u32 v240, v225, 4, v240\n\t"
    "s_lshl_b32 s79, s66, 12\n\t"
    "s_lshl_b32 s75, s70, 18\n\t"
    "s_add_u32 s75, s75, s79\n\t"
    "s_add_u32 s75, s75, 65536\n\t"
    "s_add_u32 s79, s79, 98304\n\t"
    "v_add_u32 v211, s79, v240\n\t"
    "v_lshlrev_b32 v230, 4, v255\n\t"
    "s_and_b32 s25, s25, 0xffff\n\t"
    "s_and_b32 s29, s29, 0xffff\n\t"
    "s_and_b32 s37, s37, 0xffff\n\t"
    "s_and_b32 s21, s21, 0xffff\n\t"
    "s_and_b32 s45, s45, 0xffff\n\t"
    "s_bfe_u32 s71, s52, 0x40004\n\t"
    "s_mul_i32 s59, s71, 917504\n\t"
    "s_add_u32 s59, s59, s73\n\t"
    "s_mul_i32 s60, s71, 14336\n\t"
    "s_add_u32 s60, s60, s74\n\t"
    "s_lshl_b32 s61, s71, 7\n\t"
    "s_barrier\n\t"
    "buffer_load_dwordx2 v[192:193], v208, s[24:27], s60 offen\n\t"
    "buffer_load_dwordx2 v[194:195], v208, s[28:31], s60 offen\n\t"
    "buffer_load_dwordx4 v[196:199], v209, s[36:39], s61 offen\n\t"
    "buffer_load_dwordx4 v[128:131], v200, s[20:23], s59 offen\n\t"
    "buffer_load_dwordx4 v[132:135], v201, s[20:23], s59 offen\n\t"
    "buffer_load_dwordx4 v[136:139], v202, s[20:23], s59 offen\n\t"
    "buffer_load_dwordx4 v[140:143], v203, s[20:23], s59 offen\n\t"
    "buffer_load_dwordx4 v[144:147], v204, s[20:23], s59 offen\n\t"
    "s_mov_b32 s76, 0\n\t"
    "s_mov_b32 s92, 0\n\t"
    "s_bfe_u32 s54, s52, 262144\n\t"
    "s_bitset1_b32 s76, s54\n\t"
    "s_bfe_u32 s54, s52, 262148\n\t"
    "s_bitset1_b32 s76, s54\n\t"
    "s_bfe_u32 s54, s52, 262152\n\t"
    "s_bitset1_b32 s76, s54\n\t"
    "s_lshl_b32 s58, s71, 7\n\t"
    "s_add_u32 s58, s58, s72\n\t"
    "s_add_u32 s63, s78, 32768\n\t"
    "s_waitcnt vmcnt(8)\n\t"
    "s_barrier\n\t"
    "buffer_load_dwordx4 v[148:151], v205, s[20:23], s59 offen\n\t"
    "buffer_load_dwordx4 v[152:155], v206, s[20:23], s59 offen\n\t"
    "buffer_load_dwordx4 v[156:159], v207, s[20:23], s59 offen\n\t"
    "s_bfe_u32 s71, s52, 0x40008\n\t"
    "s_mul_i32 s59, s71, 917504\n\t"
    "s_add_u32 s59, s59, s73\n\t"
    "s_mul_i32 s60, s71, 14336\n\t"
    "s_add_u32 s60, s60, s74\n\t"
    "s_lshl_b32 s61, s71, 7\n\t"
    "buffer_load_dwordx2 v[32:33], v208, s[24:27], s60 offen\n\t"
    "buffer_load_dwordx2 v[34:35], v208, s[28:31], s60 offen\n\t"
    "buffer_load_dwordx4 v[36:39], v209, s[36:39], s61 offen\n\t"
    "buffer_load_dwordx4 v[0:3], v200, s[20:23], s59 offen\n\t"
    "buffer_load_dwordx4 v[4:7], v201, s[20:23], s59 offen\n\t"
    "buffer_load_dwordx4 v[8:11], v202, s[20:23], s59 offen\n\t"
    "buffer_load_dwordx4 v[12:15], v203, s[20:23], s59 offen\n\t"
    "buffer_load_dwordx4 v[16:19], v204, s[20:23], s59 offen\n\t"
    "buffer_load_dwordx4 v[20:23], v205, s[20:23], s59 offen\n\t"
    "buffer_load_dwordx4 v[24:27], v206, s[20:23], s59 offen\n\t"
    "buffer_load_dwordx4 v[28:31], v207, s[20:23], s59 offen\n\t"
    "s_waitcnt vmcnt(19)\n\t"
    "v_lshlrev_b32 v176, 16, v192\n\t"
    "v_lshlrev_b32 v180, 16, v194\n\t"
    "v_and_b32 v177, 0xffff0000, v192\n\t"
    "v_and_b32 v181, 0xffff0000, v194\n\t"
    "v_lshlrev_b32 v178, 16, v193\n\t"
    "v_lshlrev_b32 v182, 16, v195\n\t"
    "v_and_b32 v179, 0xffff0000, v193\n\t"
    "v_and_b32 v183, 0xffff0000, v195\n\t"
    "v_mul_f32 v180, -v180, v176\n\t"
    "v_mul_f32 v181, -v181, v177\n\t"
    "v_mul_f32 v182, -v182, v178\n\t"
    "v_mul_f32 v183, -v183, v179\n\t"
    "v_lshlrev_b32 v184, 16, v196\n\t"
    "v_and_b32 v185, 0xffff0000, v196\n\t"
    "v_lshlrev_b32 v186, 16, v197\n\t"
    "v_and_b32 v187, 0xffff0000, v197\n\t"
    "v_lshlrev_b32 v188, 16, v198\n\t"
    "v_and_b32 v189, 0xffff0000, v198\n\t"
    "v_lshlrev_b32 v190, 16, v199\n\t"
    "v_and_b32 v191, 0xffff0000, v199\n\t"
    "s_waitcnt vmcnt(17)\n\t"
    "v_cvt_f32_i32 v128, v128\n\t"
    "v_cvt_f32_i32 v129, v129\n\t"
    "v_cvt_f32_i32 v130, v130\n\t"
    "v_cvt_f32_i32 v131, v131\n\t"
    "v_cvt_f32_i32 v132, v132\n\t"
    "v_cvt_f32_i32 v133, v133\n\t"
    "v_cvt_f32_i32 v134, v134\n\t"
    "v_cvt_f32_i32 v135, v135\n\t"
    "v_fma_f32 v128, v128, v176, v180\n\t"
    "v_fma_f32 v129, v129, v177, v181\n\t"
    "v_fma_f32 v130, v130, v178, v182\n\t"
    "v_fma_f32 v131, v131, v179, v183\n\t"
    "v_fma_f32 v132, v132, v176, v180\n\t"
    "v_fma_f32 v133, v133, v177, v181\n\t"
    "v_fma_f32 v134, v134, v178, v182\n\t"
    "v_fma_f32 v135, v135, v179, v183\n\t"
    "v_mul_f32 v128, v128, v184\n\t"
    "v_mul_f32 v129, v129, v184\n\t"
    "v_mul_f32 v130, v130, v184\n\t"
    "v_mul_f32 v131, v131, v184\n\t"
    "v_mul_f32 v132, v132, v185\n\t"
    "v_mul_f32 v133, v133, v185\n\t"
    "v_mul_f32 v134, v134, v185\n\t"
    "v_mul_f32 v135, v135, v185\n\t"
    "v_cvt_pk_bf16_f32 v160, v128, v132\n\t"
    "v_cvt_pk_bf16_f32 v164, v129, v133\n\t"
    "v_cvt_pk_bf16_f32 v168, v130, v134\n\t"
    "v_cvt_pk_bf16_f32 v172, v131, v135\n\t"
    "s_waitcnt vmcnt(15)\n\t"
    "v_cvt_f32_i32 v136, v136\n\t"
    "v_cvt_f32_i32 v137, v137\n\t"
    "v_cvt_f32_i32 v138, v138\n\t"
    "v_cvt_f32_i32 v139, v139\n\t"
    "v_cvt_f32_i32 v140, v140\n\t"
    "v_cvt_f32_i32 v141, v141\n\t"
    "v_cvt_f32_i32 v142, v142\n\t"
    "v_cvt_f32_i32 v143, v143\n\t"
    "v_fma_f32 v136, v136, v176, v180\n\t"
    "v_fma_f32 v137, v137, v177, v181\n\t"
    "v_fma_f32 v138, v138, v178, v182\n\t"
    "v_fma_f32 v139, v139, v179, v183\n\t"
    "v_fma_f32 v140, v140, v176, v180\n\t"
    "v_fma_f32 v141, v141, v177, v181\n\t"
    "v_fma_f32 v142, v142, v178, v182\n\t"
    "v_fma_f32 v143, v143, v179, v183\n\t"
    "v_mul_f32 v136, v136, v186\n\t"
    "v_mul_f32 v137, v137, v186\n\t"
    "v_mul_f32 v138, v138, v186\n\t"
    "v_mul_f32 v139, v139, v186\n\t"
    "v_mul_f32 v140, v140, v187\n\t"
    "v_mul_f32 v141, v141, v187\n\t"
    "v_mul_f32 v142, v142, v187\n\t"
    "v_mul_f32 v143, v143, v187\n\t"
    "v_cvt_pk_bf16_f32 v161, v136, v140\n\t"
    "v_cvt_pk_bf16_f32 v165, v137, v141\n\t"
    "v_cvt_pk_bf16_f32 v169, v138, v142\n\t"
    "v_cvt_pk_bf16_f32 v173, v139, v143\n\t"
    "s_waitcnt vmcnt(13)\n\t"
    "v_cvt_f32_i32 v144, v144\n\t"
    "v_cvt_f32_i32 v145, v145\n\t"
    "v_cvt_f32_i32 v146, v146\n\t"
    "v_cvt_f32_i32 v147, v147\n\t"
    "v_cvt_f32_i32 v148, v148\n\t"
    "v_cvt_f32_i32 v149, v149\n\t"
    "v_cvt_f32_i32 v150, v150\n\t"
    "v_cvt_f32_i32 v151, v151\n\t"
    "v_fma_f32 v144, v144, v176, v180\n\t"
    "v_fma_f32 v145, v145, v177, v181\n\t"
    "v_fma_f32 v146, v146, v178, v182\n\t"
    "v_fma_f32 v147, v147, v179, v183\n\t"
    "v_fma_f32 v148, v148, v176, v180\n\t"
    "v_fma_f32 v149, v149, v177, v181\n\t"
    "v_fma_f32 v150, v150, v178, v182\n\t"
    "v_fma_f32 v151, v151, v179, v183\n\t"
    "v_mul_f32 v144, v144, v188\n\t"
    "v_mul_f32 v145, v145, v188\n\t"
    "v_mul_f32 v146, v146, v188\n\t"
    "v_mul_f32 v147, v147, v188\n\t"
    "v_mul_f32 v148, v148, v189\n\t"
    "v_mul_f32 v149, v149, v189\n\t"
    "v_mul_f32 v150, v150, v189\n\t"
    "v_mul_f32 v151, v151, v189\n\t"
    "v_cvt_pk_bf16_f32 v162, v144, v148\n\t"
    "v_cvt_pk_bf16_f32 v166, v145, v149\n\t"
    "v_cvt_pk_bf16_f32 v170, v146, v150\n\t"
    "v_cvt_pk_bf16_f32 v174, v147, v151\n\t"
    "s_waitcnt vmcnt(11)\n\t"
    "v_cvt_f32_i32 v152, v152\n\t"
    "v_cvt_f32_i32 v153, v153\n\t"
    "v_cvt_f32_i32 v154, v154\n\t"
    "v_cvt_f32_i32 v155, v155\n\t"
    "v_cvt_f32_i32 v156, v156\n\t"
    "v_cvt_f32_i32 v157, v157\n\t"
    "v_cvt_f32_i32 v158, v158\n\t"
    "v_cvt_f32_i32 v159, v159\n\t"
    "v_fma_f32 v152, v152, v176, v180\n\t"
    "v_fma_f32 v153, v153, v177, v181\n\t"
    "v_fma_f32 v154, v154, v178, v182\n\t"
    "v_fma_f32 v155, v155, v179, v183\n\t"
    "v_fma_f32 v156, v156, v176, v180\n\t"
    "v_fma_f32 v157, v157, v177, v181\n\t"
    "v_fma_f32 v158, v158, v178, v182\n\t"
    "v_fma_f32 v159, v159, v179, v183\n\t"
    "v_mul_f32 v152, v152, v190\n\t"
    "v_mul_f32 v153, v153, v190\n\t"
    "v_mul_f32 v154, v154, v190\n\t"
    "v_mul_f32 v155, v155, v190\n\t"
    "v_mul_f32 v156, v156, v191\n\t"
    "v_mul_f32 v157, v157, v191\n\t"
    "v_mul_f32 v158, v158, v191\n\t"
    "v_mul_f32 v159, v159, v191\n\t"
    "v_cvt_pk_bf16_f32 v163, v152, v156\n\t"
    "v_cvt_pk_bf16_f32 v167, v153, v157\n\t"
    "v_cvt_pk_bf16_f32 v171, v154, v158\n\t"
    "v_cvt_pk_bf16_f32 v175, v155, v159\n\t"
    "ds_write_b128 v211, v[160:163] offset:16384\n\t"
    "ds_write_b128 v211, v[164:167] offset:16896\n\t"
    "ds_write_b128 v211, v[168:171] offset:17408\n\t"
    "ds_write_b128 v211, v[172:175] offset:17920\n\t"
    "s_mov_b32 m0, s63\n\t"
    "s_nop 0\n\t"
    "buffer_load_dwordx4 v216, s[16:19], s58 offen lds\n\t"
    "buffer_load_dwordx4 v217, s[16:19], s58 offen offset:1024 lds\n\t"
    "buffer_load_dwordx4 v218, s[16:19], s58 offen offset:2048 lds\n\t"
    "buffer_load_dwordx4 v219, s[16:19], s58 offen offset:3072 lds\n\t"
    "s_add_u32 m0, s63, 4096\n\t"
    "s_nop 0\n\t"
    "buffer_load_dwordx4 v220, s[16:19], s58 offen lds\n\t"
    "buffer_load_dwordx4 v221, s[16:19], s58 offen offset:1024 lds\n\t"
    "buffer_load_dwordx4 v222, s[16:19], s58 offen offset:2048 lds\n\t"
    "buffer_load_dwordx4 v223, s[16:19], s58 offen offset:3072 lds\n\t"
    "s_bfe_u32 s71, s52, 262152\n\t"
    "s_lshl_b32 s58, s71, 7\n\t"
    "s_add_u32 s58, s58, s72\n\t"
    "s_add_u32 s63, s78, 65536\n\t"
    "s_waitcnt vmcnt(16)\n\t"
    "v_lshlrev_b32 v176, 16, v32\n\t"
    "v_lshlrev_b32 v180, 16, v34\n\t"
    "v_and_b32 v177, 0xffff0000, v32\n\t"
    "v_and_b32 v181, 0xffff0000, v34\n\t"
    "v_lshlrev_b32 v178, 16, v33\n\t"
    "v_lshlrev_b32 v182, 16, v35\n\t"
    "v_and_b32 v179, 0xffff0000, v33\n\t"
    "v_and_b32 v183, 0xffff0000, v35\n\t"
    "v_mul_f32 v180, -v180, v176\n\t"
    "v_mul_f32 v181, -v181, v177\n\t"
    "v_mul_f32 v182, -v182, v178\n\t"
    "v_mul_f32 v183, -v183, v179\n\t"
    "v_lshlrev_b32 v184, 16, v36\n\t"
    "v_and_b32 v185, 0xffff0000, v36\n\t"
    "v_lshlrev_b32 v186, 16, v37\n\t"
    "v_and_b32 v187, 0xffff0000, v37\n\t"
    "v_lshlrev_b32 v188, 16, v38\n\t"
    "v_and_b32 v189, 0xffff0000, v38\n\t"
    "v_lshlrev_b32 v190, 16, v39\n\t"
    "v_and_b32 v191, 0xffff0000, v39\n\t"
    "s_waitcnt vmcnt(14)\n\t"
    "v_cvt_f32_i32 v0, v0\n\t"
    "v_cvt_f32_i32 v1, v1\n\t"
    "v_cvt_f32_i32 v2, v2\n\t"
    "v_cvt_f32_i32 v3, v3\n\t"
    "v_cvt_f32_i32 v4, v4\n\t"
    "v_cvt_f32_i32 v5, v5\n\t"
    "v_cvt_f32_i32 v6, v6\n\t"
    "v_cvt_f32_i32 v7, v7\n\t"
    "v_fma_f32 v0, v0, v176, v180\n\t"
    "v_fma_f32 v1, v1, v177, v181\n\t"
    "v_fma_f32 v2, v2, v178, v182\n\t"
    "v_fma_f32 v3, v3, v179, v183\n\t"
    "v_fma_f32 v4, v4, v176, v180\n\t"
    "v_fma_f32 v5, v5, v177, v181\n\t"
    "v_fma_f32 v6, v6, v178, v182\n\t"
    "v_fma_f32 v7, v7, v179, v183\n\t"
    "v_mul_f32 v0, v0, v184\n\t"
    "v_mul_f32 v1, v1, v184\n\t"
    "v_mul_f32 v2, v2, v184\n\t"
    "v_mul_f32 v3, v3, v184\n\t"
    "v_mul_f32 v4, v4, v185\n\t"
    "v_mul_f32 v5, v5, v185\n\t"
    "v_mul_f32 v6, v6, v185\n\t"
    "v_mul_f32 v7, v7, v185\n\t"
    "v_cvt_pk_bf16_f32 v160, v0, v4\n\t"
    "v_cvt_pk_bf16_f32 v164, v1, v5\n\t"
    "v_cvt_pk_bf16_f32 v168, v2, v6\n\t"
    "v_cvt_pk_bf16_f32 v172, v3, v7\n\t"
    "s_waitcnt vmcnt(12)\n\t"
    "v_cvt_f32_i32 v8, v8\n\t"
    "v_cvt_f32_i32 v9, v9\n\t"
    "v_cvt_f32_i32 v10, v10\n\t"
    "v_cvt_f32_i32 v11, v11\n\t"
    "v_cvt_f32_i32 v12, v12\n\t"
    "v_cvt_f32_i32 v13, v13\n\t"
    "v_cvt_f32_i32 v14, v14\n\t"
    "v_cvt_f32_i32 v15, v15\n\t"
    "v_fma_f32 v8, v8, v176, v180\n\t"
    "v_fma_f32 v9, v9, v177, v181\n\t"
    "v_fma_f32 v10, v10, v178, v182\n\t"
    "v_fma_f32 v11, v11, v179, v183\n\t"
    "v_fma_f32 v12, v12, v176, v180\n\t"
    "v_fma_f32 v13, v13, v177, v181\n\t"
    "v_fma_f32 v14, v14, v178, v182\n\t"
    "v_fma_f32 v15, v15, v179, v183\n\t"
    "v_mul_f32 v8, v8, v186\n\t"
    "v_mul_f32 v9, v9, v186\n\t"
    "v_mul_f32 v10, v10, v186\n\t"
    "v_mul_f32 v11, v11, v186\n\t"
    "v_mul_f32 v12, v12, v187\n\t"
    "v_mul_f32 v13, v13, v187\n\t"
    "v_mul_f32 v14, v14, v187\n\t"
    "v_mul_f32 v15, v15, v187\n\t"
    "v_cvt_pk_bf16_f32 v161, v8, v12\n\t"
    "v_cvt_pk_bf16_f32 v165, v9, v13\n\t"
    "v_cvt_pk_bf16_f32 v169, v10, v14\n\t"
    "v_cvt_pk_bf16_f32 v173, v11, v15\n\t"
    "s_waitcnt vmcnt(10)\n\t"
    "v_cvt_f32_i32 v16, v16\n\t"
    "v_cvt_f32_i32 v17, v17\n\t"
    "v_cvt_f32_i32 v18, v18\n\t"
    "v_cvt_f32_i32 v19, v19\n\t"
    "v_cvt_f32_i32 v20, v20\n\t"
    "v_cvt_f32_i32 v21, v21\n\t"
    "v_cvt_f32_i32 v22, v22\n\t"
    "v_cvt_f32_i32 v23, v23\n\t"
    "v_fma_f32 v16, v16, v176, v180\n\t"
    "v_fma_f32 v17, v17, v177, v181\n\t"
    "v_fma_f32 v18, v18, v178, v182\n\t"
    "v_fma_f32 v19, v19, v179, v183\n\t"
    "v_fma_f32 v20, v20, v176, v180\n\t"
    "v_fma_f32 v21, v21, v177, v181\n\t"
    "v_fma_f32 v22, v22, v178, v182\n\t"
    "v_fma_f32 v23, v23, v179, v183\n\t"
    "v_mul_f32 v16, v16, v188\n\t"
    "v_mul_f32 v17, v17, v188\n\t"
    "v_mul_f32 v18, v18, v188\n\t"
    "v_mul_f32 v19, v19, v188\n\t"
    "v_mul_f32 v20, v20, v189\n\t"
    "v_mul_f32 v21, v21, v189\n\t"
    "v_mul_f32 v22, v22, v189\n\t"
    "v_mul_f32 v23, v23, v189\n\t"
    "v_cvt_pk_bf16_f32 v162, v16, v20\n\t"
    "v_cvt_pk_bf16_f32 v166, v17, v21\n\t"
    "v_cvt_pk_bf16_f32 v170, v18, v22\n\t"
    "v_cvt_pk_bf16_f32 v174, v19, v23\n\t"
    "s_waitcnt vmcnt(8)\n\t"
    "v_cvt_f32_i32 v24, v24\n\t"
    "v_cvt_f32_i32 v25, v25\n\t"
    "v_cvt_f32_i32 v26, v26\n\t"
    "v_cvt_f32_i32 v27, v27\n\t"
    "v_cvt_f32_i32 v28, v28\n\t"
    "v_cvt_f32_i32 v29, v29\n\t"
    "v_cvt_f32_i32 v30, v30\n\t"
    "v_cvt_f32_i32 v31, v31\n\t"
    "v_fma_f32 v24, v24, v176, v180\n\t"
    "v_fma_f32 v25, v25, v177, v181\n\t"
    "v_fma_f32 v26, v26, v178, v182\n\t"
    "v_fma_f32 v27, v27, v179, v183\n\t"
    "v_fma_f32 v28, v28, v176, v180\n\t"
    "v_fma_f32 v29, v29, v177, v181\n\t"
    "v_fma_f32 v30, v30, v178, v182\n\t"
    "v_fma_f32 v31, v31, v179, v183\n\t"
    "v_mul_f32 v24, v24, v190\n\t"
    "v_mul_f32 v25, v25, v190\n\t"
    "v_mul_f32 v26, v26, v190\n\t"
    "v_mul_f32 v27, v27, v190\n\t"
    "v_mul_f32 v28, v28, v191\n\t"
    "v_mul_f32 v29, v29, v191\n\t"
    "v_mul_f32 v30, v30, v191\n\t"
    "v_mul_f32 v31, v31, v191\n\t"
    "v_cvt_pk_bf16_f32 v163, v24, v28\n\t"
    "v_cvt_pk_bf16_f32 v167, v25, v29\n\t"
    "v_cvt_pk_bf16_f32 v171, v26, v30\n\t"
    "v_cvt_pk_bf16_f32 v175, v27, v31\n\t"
    "ds_write_b128 v211, v[160:163] offset:32768\n\t"
    "ds_write_b128 v211, v[164:167] offset:33280\n\t"
    "ds_write_b128 v211, v[168:171] offset:33792\n\t"
    "ds_write_b128 v211, v[172:175] offset:34304\n\t"
    "s_mov_b32 m0, s63\n\t"
    "s_nop 0\n\t"
    "buffer_load_dwordx4 v216, s[16:19], s58 offen lds\n\t"
    "buffer_load_dwordx4 v217, s[16:19], s58 offen offset:1024 lds\n\t"
    "buffer_load_dwordx4 v218, s[16:19], s58 offen offset:2048 lds\n\t"
    "buffer_load_dwordx4 v219, s[16:19], s58 offen offset:3072 lds\n\t"
    "s_add_u32 m0, s63, 4096\n\t"
    "s_nop 0\n\t"
    "buffer_load_dwordx4 v220, s[16:19], s58 offen lds\n\t"
    "buffer_load_dwordx4 v221, s[16:19], s58 offen offset:1024 lds\n\t"
    "buffer_load_dwordx4 v222, s[16:19], s58 offen offset:2048 lds\n\t"
    "buffer_load_dwordx4 v223, s[16:19], s58 offen offset:3072 lds\n\t"
    "s_waitcnt vmcnt(8) lgkmcnt(0)\n\t"
    "s_barrier\n\t"
    "buffer_load_dword v214, v212, s[48:51], 0 offen sc1\n\t"
    "s_bfe_u32 s71, s52, 262156\n\t"
    "s_lshl_b32 s58, s71, 7\n\t"
    "s_add_u32 s58, s58, s72\n\t"
    "s_add_u32 s63, s78, 0\n\t"
    "s_mov_b32 m0, s63\n\t"
    "s_nop 0\n\t"
    "buffer_load_dwordx4 v216, s[16:19], s58 offen lds\n\t"
    "buffer_load_dwordx4 v217, s[16:19], s58 offen offset:1024 lds\n\t"
    "buffer_load_dwordx4 v218, s[16:19], s58 offen offset:2048 lds\n\t"
    "buffer_load_dwordx4 v219, s[16:19], s58 offen offset:3072 lds\n\t"
    "s_add_u32 m0, s63, 4096\n\t"
    "s_nop 0\n\t"
    "buffer_load_dwordx4 v220, s[16:19], s58 offen lds\n\t"
    "buffer_load_dwordx4 v221, s[16:19], s58 offen offset:1024 lds\n\t"
    "buffer_load_dwordx4 v222, s[16:19], s58 offen offset:2048 lds\n\t"
    "buffer_load_dwordx4 v223, s[16:19], s58 offen offset:3072 lds\n\t"
    "s_mov_b32 s65, 0\n\t"
    "s_mov_b32 s83, 0\n\t"
    "s_waitcnt vmcnt(8) lgkmcnt(0)\n\t"
    "v_cmp_ne_u32 vcc, v214, v213\n\t"
    "s_mov_b32 s82, 0\n\t"
    "s_nop 2\n\t"
    "s_and_b32 s54, vcc_lo, 65535\n\t"
    "s_or_b32 s76, s76, s54\n\t"
    "s_bitcmp1_b32 s76, s71\n\t"
    "s_cbranch_scc0 Lpf1%=\n\t"
    "s_lshr_b32 s56, s65, 1\n\t"
    "s_add_u32 m0, s56, s79\n\t"
    "s_lshl_b32 s62, s71, 14\n\t"
    "s_add_u32 s62, s62, s75\n\t"
    "buffer_load_dwordx4 v230, s[44:47], s62 offen sc1 lds\n\t"
    "buffer_load_dwordx4 v230, s[44:47], s62 offen offset:1024 sc1 lds\n\t"
    "buffer_load_dwordx4 v230, s[44:47], s62 offen offset:2048 sc1 lds\n\t"
    "buffer_load_dwordx4 v230, s[44:47], s62 offen offset:3072 sc1 lds\n\t"
    "s_mov_b32 s83, 1\n\t"
    "Lpf1%=:\n\t"
    "buffer_load_dword v214, v212, s[48:51], 0 offen sc1\n\t"
    "s_mov_b32 s82, 1\n\t"
    "s_barrier\n\t"
    "s_lshr_b64 s[52:53], s[52:53], 12\n\t"
    "s_mov_b32 s64, 3\n\t"
    "s_mov_b32 s65, 0\n\t"
    "s_mov_b32 s77, 3\n\t"
    "Lploop%=:\n\t"
    "s_cmp_lg_u32 s83, 0\n\t"
    "s_cbranch_scc1 Lwx%=\n\t"
    "s_and_b32 s71, s52, 15\n\t"
    "Lchk%=:\n\t"
    "s_bitcmp1_b32 s76, s71\n\t"
    "s_cbranch_scc0 Lwait%=\n\t"
    "s_lshr_b32 s56, s65, 1\n\t"
    "s_add_u32 m0, s56, s79\n\t"
    "s_lshl_b32 s62, s71, 14\n\t"
    "s_add_u32 s62, s62, s75\n\t"
    "buffer_load_dwordx4 v230, s[44:47], s62 offen sc1 lds\n\t"
    "buffer_load_dwordx4 v230, s[44:47], s62 offen offset:1024 sc1 lds\n\t"
    "buffer_load_dwordx4 v230, s[44:47], s62 offen offset:2048 sc1 lds\n\t"
    "buffer_load_dwordx4 v230, s[44:47], s62 offen offset:3072 sc1 lds\n\t"
    "Lwx%=:\n\t"
    "s_cmp_eq_u32 s64, 15\n\t"
    "s_cbranch_scc1 Lplast%=\n\t"
    "s_lshr_b64 s[52:53], s[52:53], 4\n\t"
    "s_add_u32 s64, s64, 1\n\t"
    "s_and_b32 s71, s52, 15\n\t"
    "s_lshl_b32 s58, s71, 7\n\t"
    "s_add_u32 s58, s58, s72\n\t"
    "s_add_u32 s65, s65, 32768\n\t"
    "s_cmp_eq_u32 s65, 98304\n\t"
    "s_cselect_b32 s65, 0, s65\n\t"
    "s_add_u32 s63, s78, s65\n\t"
    "s_bitcmp1_b32 s76, s71\n\t"
    "s_cselect_b32 s83, 1, 0\n\t"
    "s_mov_b32 m0, s63\n\t"
    "s_nop 0\n\t"
    "buffer_load_dwordx4 v216, s[16:19], s58 offen lds\n\t"
    "buffer_load_dwordx4 v217, s[16:19], s58 offen offset:1024 lds\n\t"
    "buffer_load_dwordx4 v218, s[16:19], s58 offen offset:2048 lds\n\t"
    "buffer_load_dwordx4 v219, s[16:19], s58 offen offset:3072 lds\n\t"
    "s_add_u32 m0, s63, 4096\n\t"
    "s_nop 0\n\t"
    "buffer_load_dwordx4 v220, s[16:19], s58 offen lds\n\t"
    "buffer_load_dwordx4 v221, s[16:19], s58 offen offset:1024 lds\n\t"
    "buffer_load_dwordx4 v222, s[16:19], s58 offen offset:2048 lds\n\t"
    "buffer_load_dwordx4 v223, s[16:19], s58 offen offset:3072 lds\n\t"
    "s_mov_b32 s55, 0\n\t"
    "s_bfe_u32 s54, s52, 262144\n\t"
    "s_bitset1_b32 s55, s54\n\t"
    "s_bfe_u32 s54, s52, 262148\n\t"
    "s_bitset1_b32 s55, s54\n\t"
    "s_bfe_u32 s54, s52, 262152\n\t"
    "s_bitset1_b32 s55, s54\n\t"
    "s_bfe_u32 s54, s52, 262156\n\t"
    "s_bitset1_b32 s55, s54\n\t"
    "s_cmp_eq_u32 s83, 0\n\t"
    "s_cbranch_scc1 Lnw%=\n\t"
    "s_lshr_b32 s56, s65, 1\n\t"
    "s_add_u32 m0, s56, s79\n\t"
    "s_lshl_b32 s62, s71, 14\n\t"
    "s_add_u32 s62, s62, s75\n\t"
    "buffer_load_dwordx4 v230, s[44:47], s62 offen sc1 lds\n\t"
    "buffer_load_dwordx4 v230, s[44:47], s62 offen offset:1024 sc1 lds\n\t"
    "buffer_load_dwordx4 v230, s[44:47], s62 offen offset:2048 sc1 lds\n\t"
    "buffer_load_dwordx4 v230, s[44:47], s62 offen offset:3072 sc1 lds\n\t"
    "s_waitcnt vmcnt(12) lgkmcnt(0)\n\t"
    "s_branch Lnw2%=\n\t"
    "Lnw%=:\n\t"
    "s_waitcnt vmcnt(8) lgkmcnt(0)\n\t"
    "Lnw2%=:\n\t"
    "s_cmp_eq_u32 s82, 0\n\t"
    "s_cbranch_scc1 Lne%=\n\t"
    "v_cmp_ne_u32 vcc, v214, v213\n\t"
    "s_mov_b32 s82, 0\n\t"
    "s_nop 2\n\t"
    "s_and_b32 s54, vcc_lo, 65535\n\t"
    "s_or_b32 s76, s76, s54\n\t"
    "Lne%=:\n\t"
    "s_andn2_b32 s55, s55, s76\n\t"
    "s_cbranch_scc0 Lnpl%=\n\t"
    "buffer_load_dword v214, v212, s[48:51], 0 offen sc1\n\t"
    "s_mov_b32 s82, 1\n\t"
    "Lnpl%=:\n\t"
    "s_barrier\n\t"
    "s_branch Lploop%=\n\t"
    "Lplast%=:\n\t"
    "s_waitcnt vmcnt(0) lgkmcnt(0)\n\t"
    "s_barrier\n\t"
    "s_branch Lend%=\n\t"
    "Lwait%=:\n\t"
    "s_cmp_lg_u32 s82, 0\n\t"
    "s_cbranch_scc1 Lw0%=\n\t"
    "s_cmp_eq_u32 s77, 0\n\t"
    "s_cbranch_scc1 Lself%=\n\t"
    "s_sub_u32 s77, s77, 1\n\t"
    "buffer_load_dword v214, v212, s[48:51], 0 offen sc1\n\t"
    "Lw0%=:\n\t"
    "s_waitcnt vmcnt(0)\n\t"
    "v_cmp_ne_u32 vcc, v214, v213\n\t"
    "s_mov_b32 s82, 0\n\t"
    "s_nop 2\n\t"
    "s_and_b32 s54, vcc_lo, 65535\n\t"
    "s_or_b32 s76, s76, s54\n\t"
    "s_branch Lchk%=\n\t"
    "Lself%=:\n\t"
    "s_mul_i32 s59, s71, 917504\n\t"
    "s_add_u32 s59, s59, s73\n\t"
    "s_mul_i32 s60, s71, 14336\n\t"
    "s_add_u32 s60, s60, s74\n\t"
    "s_lshl_b32 s61, s71, 7\n\t"
    "buffer_load_dwordx2 v[192:193], v208, s[24:27], s60 offen\n\t"
    "buffer_load_dwordx2 v[194:195], v208, s[28:31], s60 offen\n\t"
    "buffer_load_dwordx4 v[196:199], v209, s[36:39], s61 offen\n\t"
    "buffer_load_dwordx4 v[128:131], v200, s[20:23], s59 offen nt\n\t"
    "buffer_load_dwordx4 v[132:135], v201, s[20:23], s59 offen nt\n\t"
    "buffer_load_dwordx4 v[136:139], v202, s[20:23], s59 offen nt\n\t"
    "buffer_load_dwordx4 v[140:143], v203, s[20:23], s59 offen nt\n\t"
    "buffer_load_dwordx4 v[144:147], v204, s[20:23], s59 offen nt\n\t"
    "buffer_load_dwordx4 v[148:151], v205, s[20:23], s59 offen nt\n\t"
    "buffer_load_dwordx4 v[152:155], v206, s[20:23], s59 offen nt\n\t"
    "buffer_load_dwordx4 v[156:159], v207, s[20:23], s59 offen nt\n\t"
    "s_waitcnt vmcnt(8)\n\t"
    "v_lshlrev_b32 v176, 16, v192\n\t"
    "v_lshlrev_b32 v180, 16, v194\n\t"
    "v_and_b32 v177, 0xffff0000, v192\n\t"
    "v_and_b32 v181, 0xffff0000, v194\n\t"
    "v_lshlrev_b32 v178, 16, v193\n\t"
    "v_lshlrev_b32 v182, 16, v195\n\t"
    "v_and_b32 v179, 0xffff0000, v193\n\t"
    "v_and_b32 v183, 0xffff0000, v195\n\t"
    "v_mul_f32 v180, -v180, v176\n\t"
    "v_mul_f32 v181, -v181, v177\n\t"
    "v_mul_f32 v182, -v182, v178\n\t"
    "v_mul_f32 v183, -v183, v179\n\t"
    "v_lshlrev_b32 v184, 16, v196\n\t"
    "v_and_b32 v185, 0xffff0000, v196\n\t"
    "v_lshlrev_b32 v186, 16, v197\n\t"
    "v_and_b32 v187, 0xffff0000, v197\n\t"
    "v_lshlrev_b32 v188, 16, v198\n\t"
    "v_and_b32 v189, 0xffff0000, v198\n\t"
    "v_lshlrev_b32 v190, 16, v199\n\t"
    "v_and_b32 v191, 0xffff0000, v199\n\t"
    "s_waitcnt vmcnt(6)\n\t"
    "v_cvt_f32_i32 v128, v128\n\t"
    "v_cvt_f32_i32 v129, v129\n\t"
    "v_cvt_f32_i32 v130, v130\n\t"
    "v_cvt_f32_i32 v131, v131\n\t"
    "v_cvt_f32_i32 v132, v132\n\t"
    "v_cvt_f32_i32 v133, v133\n\t"
    "v_cvt_f32_i32 v134, v134\n\t"
    "v_cvt_f32_i32 v135, v135\n\t"
    "v_fma_f32 v128, v128, v176, v180\n\t"
    "v_fma_f32 v129, v129, v177, v181\n\t"
    "v_fma_f32 v130, v130, v178, v182\n\t"
    "v_fma_f32 v131, v131, v179, v183\n\t"
    "v_fma_f32 v132, v132, v176, v180\n\t"
    "v_fma_f32 v133, v133, v177, v181\n\t"
    "v_fma_f32 v134, v134, v178, v182\n\t"
    "v_fma_f32 v135, v135, v179, v183\n\t"
    "v_mul_f32 v128, v128, v184\n\t"
    "v_mul_f32 v129, v129, v184\n\t"
    "v_mul_f32 v130, v130, v184\n\t"
    "v_mul_f32 v131, v131, v184\n\t"
    "v_mul_f32 v132, v132, v185\n\t"
    "v_mul_f32 v133, v133, v185\n\t"
    "v_mul_f32 v134, v134, v185\n\t"
    "v_mul_f32 v135, v135, v185\n\t"
    "v_cvt_pk_bf16_f32 v160, v128, v132\n\t"
    "v_cvt_pk_bf16_f32 v164, v129, v133\n\t"
    "v_cvt_pk_bf16_f32 v168, v130, v134\n\t"
    "v_cvt_pk_bf16_f32 v172, v131, v135\n\t"
    "s_waitcnt vmcnt(4)\n\t"
    "v_cvt_f32_i32 v136, v136\n\t"
    "v_cvt_f32_i32 v137, v137\n\t"
    "v_cvt_f32_i32 v138, v138\n\t"
    "v_cvt_f32_i32 v139, v139\n\t"
    "v_cvt_f32_i32 v140, v140\n\t"
    "v_cvt_f32_i32 v141, v141\n\t"
    "v_cvt_f32_i32 v142, v142\n\t"
    "v_cvt_f32_i32 v143, v143\n\t"
    "v_fma_f32 v136, v136, v176, v180\n\t"
    "v_fma_f32 v137, v137, v177, v181\n\t"
    "v_fma_f32 v138, v138, v178, v182\n\t"
    "v_fma_f32 v139, v139, v179, v183\n\t"
    "v_fma_f32 v140, v140, v176, v180\n\t"
    "v_fma_f32 v141, v141, v177, v181\n\t"
    "v_fma_f32 v142, v142, v178, v182\n\t"
    "v_fma_f32 v143, v143, v179, v183\n\t"
    "v_mul_f32 v136, v136, v186\n\t"
    "v_mul_f32 v137, v137, v186\n\t"
    "v_mul_f32 v138, v138, v186\n\t"
    "v_mul_f32 v139, v139, v186\n\t"
    "v_mul_f32 v140, v140, v187\n\t"
    "v_mul_f32 v141, v141, v187\n\t"
    "v_mul_f32 v142, v142, v187\n\t"
    "v_mul_f32 v143, v143, v187\n\t"
    "v_cvt_pk_bf16_f32 v161, v136, v140\n\t"
    "v_cvt_pk_bf16_f32 v165, v137, v141\n\t"
    "v_cvt_pk_bf16_f32 v169, v138, v142\n\t"
    "v_cvt_pk_bf16_f32 v173, v139, v143\n\t"
    "s_waitcnt vmcnt(2)\n\t"
    "v_cvt_f32_i32 v144, v144\n\t"
    "v_cvt_f32_i32 v145, v145\n\t"
    "v_cvt_f32_i32 v146, v146\n\t"
    "v_cvt_f32_i32 v147, v147\n\t"
    "v_cvt_f32_i32 v148, v148\n\t"
    "v_cvt_f32_i32 v149, v149\n\t"
    "v_cvt_f32_i32 v150, v150\n\t"
    "v_cvt_f32_i32 v151, v151\n\t"
    "v_fma_f32 v144, v144, v176, v180\n\t"
    "v_fma_f32 v145, v145, v177, v181\n\t"
    "v_fma_f32 v146, v146, v178, v182\n\t"
    "v_fma_f32 v147, v147, v179, v183\n\t"
    "v_fma_f32 v148, v148, v176, v180\n\t"
    "v_fma_f32 v149, v149, v177, v181\n\t"
    "v_fma_f32 v150, v150, v178, v182\n\t"
    "v_fma_f32 v151, v151, v179, v183\n\t"
    "v_mul_f32 v144, v144, v188\n\t"
    "v_mul_f32 v145, v145, v188\n\t"
    "v_mul_f32 v146, v146, v188\n\t"
    "v_mul_f32 v147, v147, v188\n\t"
    "v_mul_f32 v148, v148, v189\n\t"
    "v_mul_f32 v149, v149, v189\n\t"
    "v_mul_f32 v150, v150, v189\n\t"
    "v_mul_f32 v151, v151, v189\n\t"
    "v_cvt_pk_bf16_f32 v162, v144, v148\n\t"
    "v_cvt_pk_bf16_f32 v166, v145, v149\n\t"
    "v_cvt_pk_bf16_f32 v170, v146, v150\n\t"
    "v_cvt_pk_bf16_f32 v174, v147, v151\n\t"
    "s_waitcnt vmcnt(0)\n\t"
    "v_cvt_f32_i32 v152, v152\n\t"
    "v_cvt_f32_i32 v153, v153\n\t"
    "v_cvt_f32_i32 v154, v154\n\t"
    "v_cvt_f32_i32 v155, v155\n\t"
    "v_cvt_f32_i32 v156, v156\n\t"
    "v_cvt_f32_i32 v157, v157\n\t"
    "v_cvt_f32_i32 v158, v158\n\t"
    "v_cvt_f32_i32 v159, v159\n\t"
    "v_fma_f32 v152, v152, v176, v180\n\t"
    "v_fma_f32 v153, v153, v177, v181\n\t"
    "v_fma_f32 v154, v154, v178, v182\n\t"
    "v_fma_f32 v155, v155, v179, v183\n\t"
    "v_fma_f32 v156, v156, v176, v180\n\t"
    "v_fma_f32 v157, v157, v177, v181\n\t"
    "v_fma_f32 v158, v158, v178, v182\n\t"
    "v_fma_f32 v159, v159, v179, v183\n\t"
    "v_mul_f32 v152, v152, v190\n\t"
    "v_mul_f32 v153, v153, v190\n\t"
    "v_mul_f32 v154, v154, v190\n\t"
    "v_mul_f32 v155, v155, v190\n\t"
    "v_mul_f32 v156, v156, v191\n\t"
    "v_mul_f32 v157, v157, v191\n\t"
    "v_mul_f32 v158, v158, v191\n\t"
    "v_mul_f32 v159, v159, v191\n\t"
    "v_cvt_pk_bf16_f32 v163, v152, v156\n\t"
    "v_cvt_pk_bf16_f32 v167, v153, v157\n\t"
    "v_cvt_pk_bf16_f32 v171, v154, v158\n\t"
    "v_cvt_pk_bf16_f32 v175, v155, v159\n\t"
    "s_lshr_b32 s56, s65, 1\n\t"
    "v_add_u32 v226, s56, v211\n\t"
    "ds_write_b128 v226, v[160:163] offset:0\n\t"
    "ds_write_b128 v226, v[164:167] offset:512\n\t"
    "ds_write_b128 v226, v[168:171] offset:1024\n\t"
    "ds_write_b128 v226, v[172:175] offset:1536\n\t"
    "s_branch Lwx%=\n\t"
    "Lhelp%=:\n\t"
    "s_cmp_lt_u32 %2, 6\n\t"
    "s_cbranch_scc1 Lcp%=\n\t"
    "s_getpc_b64 s[12:13]\n\t"
    "Lpc%=:\n\t"
    "s_sub_u32 s12, s12, Lpc%=-Lcons%=\n\t"
    "s_subb_u32 s13, s13, 0\n\t"
    "s_sub_u32 s14, %2, 6\n\t"
    "s_lshl_b32 s14, s14, 13\n\t"
    "v_lshl_add_u32 v10, v255, 7, s14\n\t"
    "s_mov_b32 s14, Lend%=-Lcons%=\n\t"
    "v_cmp_gt_u32 vcc, s14, v10\n\t"
    "s_nop 1\n\t"
    "s_and_b64 exec, exec, vcc\n\t"
    "s_cbranch_execz Lcp2%=\n\t"
    "global_load_dword v11, v10, s[12:13]\n\t"
    "Lcp2%=:\n\t"
    "s_mov_b64 exec, -1\n\t"
    "Lcp%=:\n\t"
    "s_cmp_lt_u32 %2, 7\n\t"
    "s_cbranch_scc1 Lhw%=\n\t"
    "s_cmp_lg_u32 %2, 7\n\t"
    "s_cbranch_scc1 Lend%=\n\t"
    "s_add_u32 s69, s69, s55\n\t"
    "s_and_b32 s15, s69, 3\n\t"
    "s_lshl_b32 s12, s15, 2\n\t"
    "s_lshr_b32 s12, 0x64206420, s12\n\t"
    "s_and_b32 s12, s12, 0xffff\n\t"
    "s_lshr_b32 s13, s69, 2\n\t"
    "s_mul_i32 s13, s13, 0x8888\n\t"
    "s_add_u32 s13, s13, s12\n\t"
    "s_xor_b32 s14, s13, 0x8888\n\t"
    "s_lshl_b32 s14, s14, 12\n\t"
    "s_lshr_b32 s12, s13, 4\n\t"
    "s_or_b32 s12, s12, s14\n\t"
    "s_and_b32 s52, s13, 15\n\t"
    "s_mov_b32 s13, 0\n\t"
    "s_lshl_b64 s[12:13], s[12:13], 12\n\t"
    "s_or_b32 s52, s52, s12\n\t"
    "s_bitcmp1_b32 s15, 0\n\t"
    "s_mov_b32 s14, 3751499520\n\t"
    "s_cselect_b32 s14, s14, 4256789760\n\t"
    "s_mov_b32 s12, 304\n\t"
    "s_cselect_b32 s12, s12, 784\n\t"
    "s_or_b32 s53, s13, s14\n\t"
    "s_or_b32 s52, s52, s12\n\t"
    "s_sub_u32 s54, s68, 28\n\t"
    "s_lshl_b32 s54, s54, 6\n\t"
    "s_lshl_b32 s55, s69, 8\n\t"
    "s_add_u32 s54, s54, s55\n\t"
    "v_add_u32 v1, s54, v255\n\t"
    "v_lshlrev_b32 v1, 11, v1\n\t"
    "s_mov_b32 s64, 0\n\t"
    "s_waitcnt lgkmcnt(0)\n\t"
    "s_and_b32 s17, s17, 0xffff\n\t"
    "Lhx%=:\n\t"
    "s_and_b32 s12, s52, 15\n\t"
    "s_lshl_b32 s12, s12, 7\n\t"
    "buffer_load_dword v0, v1, s[16:19], s12 offen\n\t"
    "s_lshr_b64 s[52:53], s[52:53], 4\n\t"
    "s_add_u32 s64, s64, 1\n\t"
    "s_cmp_lt_u32 s64, 16\n\t"
    "s_waitcnt vmcnt(1)\n\t"
    "s_cbranch_scc1 Lhx%=\n\t"
    "s_branch Lend%=\n\t"
    "Lhw%=:\n\t"
    "v_lshrrev_b32 v224, 5, v255\n\t"
    "v_and_b32 v225, 31, v255\n\t"
    "v_lshlrev_b32 v226, 3, v224\n\t"
    "v_lshlrev_b32 v209, 1, v226\n\t"
    "v_mul_u32_u24 v227, 14336, v226\n\t"
    "v_lshl_add_u32 v200, v225, 4, v227\n\t"
    "v_add_u32 v201, 14336, v200\n\t"
    "v_add_u32 v202, 28672, v200\n\t"
    "v_add_u32 v203, 43008, v200\n\t"
    "v_add_u32 v204, 57344, v200\n\t"
    "v_add_u32 v205, 71680, v200\n\t"
    "v_add_u32 v206, 86016, v200\n\t"
    "v_add_u32 v207, 100352, v200\n\t"
    "v_lshlrev_b32 v208, 3, v225\n\t"
    "v_lshlrev_b32 v240, 11, v224\n\t"
    "v_lshl_add_u32 v240, v225, 4, v240\n\t"
    "v_mov_b32 v242, 0\n\t"
    "s_mov_b32 s92, 0\n\t"
    "s_mov_b32 s96, 0\n\t"
    "s_sub_u32 s54, s68, 28\n\t"
    "s_lshl_b32 s54, s54, 3\n\t"
    "s_add_u32 s54, s54, s67\n\t"
    "s_lshl_b32 s64, %2, 5\n\t"
    "s_add_u32 s64, s64, s54\n\t"
    "s_waitcnt lgkmcnt(0)\n\t"
    "s_and_b32 s21, s21, 0xffff\n\t"
    "s_and_b32 s25, s25, 0xffff\n\t"
    "s_and_b32 s29, s29, 0xffff\n\t"
    "s_and_b32 s37, s37, 0xffff\n\t"
    "s_and_b32 s45, s45, 0xffff\n\t"
    "s_mov_b64 s[48:49], s[44:45]\n\t"
    "s_mov_b32 s50, 65536\n\t"
    "s_mov_b32 s51, 0x20000\n\t"
    "s_sleep 30\n\t"
    "s_mul_i32 s54, s64, 18725\n\t"
    "s_lshr_b32 s54, s54, 21\n\t"
    "s_mul_i32 s55, s54, 112\n\t"
    "s_sub_u32 s55, s64, s55\n\t"
    "s_lshl_b32 s54, s54, 1\n\t"
    "s_add_u32 s54, s54, 5\n\t"
    "s_and_b32 s56, s55, 3\n\t"
    "s_lshr_b32 s57, s55, 2\n\t"
    "s_lshl_b32 s93, s55, 6\n\t"
    "s_lshl_b32 s12, s54, 2\n\t"
    "s_add_u32 s93, s93, s12\n\t"
    "s_add_u32 s12, s12, s56\n\t"
    "s_mul_i32 s59, s12, 229376\n\t"
    "s_lshl_b32 s13, s57, 9\n\t"
    "s_add_u32 s59, s59, s13\n\t"
    "s_lshl_b32 s61, s12, 5\n\t"
    "s_lshr_b32 s12, s12, 1\n\t"
    "s_mul_i32 s60, s12, 7168\n\t"
    "s_lshl_b32 s13, s57, 8\n\t"
    "s_add_u32 s60, s60, s13\n\t"
    "s_lshl_b32 s12, s57, 4\n\t"
    "s_add_u32 s12, s12, s54\n\t"
    "s_lshl_b32 s12, s12, 2\n\t"
    "s_add_u32 s12, s12, s56\n\t"
    "s_lshl_b32 s62, s12, 12\n\t"
    "s_add_u32 s62, s62, 65536\n\t"
    "s_cmp_lt_u32 s64, 672\n\t"
    "s_cselect_b32 s22, s22, 0\n\t"
    "s_cselect_b32 s26, s26, 0\n\t"
    "s_cselect_b32 s30, s30, 0\n\t"
    "s_cselect_b32 s38, s38, 0\n\t"
    "s_cselect_b32 s50, s50, 0\n\t"
    "s_add_u32 s64, s64, 224\n\t"
    "buffer_load_dword v213, v242, s[48:51], s93 offen sc1\n\t"
    "buffer_load_dwordx2 v[192:193], v208, s[24:27], s60 offen\n\t"
    "buffer_load_dwordx2 v[194:195], v208, s[28:31], s60 offen\n\t"
    "buffer_load_dwordx4 v[196:199], v209, s[36:39], s61 offen\n\t"
    "buffer_load_dwordx4 v[128:131], v200, s[20:23], s59 offen nt\n\t"
    "buffer_load_dwordx4 v[132:135], v201, s[20:23], s59 offen nt\n\t"
    "buffer_load_dwordx4 v[136:139], v202, s[20:23], s59 offen nt\n\t"
    "buffer_load_dwordx4 v[140:143], v203, s[20:23], s59 offen nt\n\t"
    "buffer_load_dwordx4 v[144:147], v204, s[20:23], s59 offen nt\n\t"
    "buffer_load_dwordx4 v[148:151], v205, s[20:23], s59 offen nt\n\t"
    "buffer_load_dwordx4 v[152:155], v206, s[20:23], s59 offen nt\n\t"
    "buffer_load_dwordx4 v[156:159], v207, s[20:23], s59 offen nt\n\t"
    "buffer_load_dword v214, v242, s[36:39], 0 offen\n\t"
    "Lht%=:\n\t"
    "s_waitcnt vmcnt(9)\n\t"
    "v_lshlrev_b32 v176, 16, v192\n\t"
    "v_lshlrev_b32 v180, 16, v194\n\t"
    "v_and_b32 v177, 0xffff0000, v192\n\t"
    "v_and_b32 v181, 0xffff0000, v194\n\t"
    "v_lshlrev_b32 v178, 16, v193\n\t"
    "v_lshlrev_b32 v182, 16, v195\n\t"
    "v_and_b32 v179, 0xffff0000, v193\n\t"
    "v_and_b32 v183, 0xffff0000, v195\n\t"
    "v_mul_f32 v180, -v180, v176\n\t"
    "v_mul_f32 v181, -v181, v177\n\t"
    "v_mul_f32 v182, -v182, v178\n\t"
    "v_mul_f32 v183, -v183, v179\n\t"
    "v_lshlrev_b32 v184, 16, v196\n\t"
    "v_and_b32 v185, 0xffff0000, v196\n\t"
    "v_lshlrev_b32 v186, 16, v197\n\t"
    "v_and_b32 v187, 0xffff0000, v197\n\t"
    "v_lshlrev_b32 v188, 16, v198\n\t"
    "v_and_b32 v189, 0xffff0000, v198\n\t"
    "v_lshlrev_b32 v190, 16, v199\n\t"
    "v_and_b32 v191, 0xffff0000, v199\n\t"
    "v_add_u32 v241, 1, v213\n\t"
    "s_mov_b32 s94, s93\n\t"
    "s_waitcnt vmcnt(7)\n\t"
    "v_cvt_f32_i32 v128, v128\n\t"
    "v_cvt_f32_i32 v129, v129\n\t"
    "v_cvt_f32_i32 v130, v130\n\t"
    "v_cvt_f32_i32 v131, v131\n\t"
    "v_cvt_f32_i32 v132, v132\n\t"
    "v_cvt_f32_i32 v133, v133\n\t"
    "v_cvt_f32_i32 v134, v134\n\t"
    "v_cvt_f32_i32 v135, v135\n\t"
    "v_fma_f32 v128, v128, v176, v180\n\t"
    "v_fma_f32 v129, v129, v177, v181\n\t"
    "v_fma_f32 v130, v130, v178, v182\n\t"
    "v_fma_f32 v131, v131, v179, v183\n\t"
    "v_fma_f32 v132, v132, v176, v180\n\t"
    "v_fma_f32 v133, v133, v177, v181\n\t"
    "v_fma_f32 v134, v134, v178, v182\n\t"
    "v_fma_f32 v135, v135, v179, v183\n\t"
    "v_mul_f32 v128, v128, v184\n\t"
    "v_mul_f32 v129, v129, v184\n\t"
    "v_mul_f32 v130, v130, v184\n\t"
    "v_mul_f32 v131, v131, v184\n\t"
    "v_mul_f32 v132, v132, v185\n\t"
    "v_mul_f32 v133, v133, v185\n\t"
    "v_mul_f32 v134, v134, v185\n\t"
    "v_mul_f32 v135, v135, v185\n\t"
    "v_cvt_pk_bf16_f32 v160, v128, v132\n\t"
    "v_cvt_pk_bf16_f32 v164, v129, v133\n\t"
    "v_cvt_pk_bf16_f32 v168, v130, v134\n\t"
    "v_cvt_pk_bf16_f32 v172, v131, v135\n\t"
    "s_waitcnt vmcnt(5)\n\t"
    "v_cvt_f32_i32 v136, v136\n\t"
    "v_cvt_f32_i32 v137, v137\n\t"
    "v_cvt_f32_i32 v138, v138\n\t"
    "v_cvt_f32_i32 v139, v139\n\t"
    "v_cvt_f32_i32 v140, v140\n\t"
    "v_cvt_f32_i32 v141, v141\n\t"
    "v_cvt_f32_i32 v142, v142\n\t"
    "v_cvt_f32_i32 v143, v143\n\t"
    "v_fma_f32 v136, v136, v176, v180\n\t"
    "v_fma_f32 v137, v137, v177, v181\n\t"
    "v_fma_f32 v138, v138, v178, v182\n\t"
    "v_fma_f32 v139, v139, v179, v183\n\t"
    "v_fma_f32 v140, v140, v176, v180\n\t"
    "v_fma_f32 v141, v141, v177, v181\n\t"
    "v_fma_f32 v142, v142, v178, v182\n\t"
    "v_fma_f32 v143, v143, v179, v183\n\t"
    "v_mul_f32 v136, v136, v186\n\t"
    "v_mul_f32 v137, v137, v186\n\t"
    "v_mul_f32 v138, v138, v186\n\t"
    "v_mul_f32 v139, v139, v186\n\t"
    "v_mul_f32 v140, v140, v187\n\t"
    "v_mul_f32 v141, v141, v187\n\t"
    "v_mul_f32 v142, v142, v187\n\t"
    "v_mul_f32 v143, v143, v187\n\t"
    "v_cvt_pk_bf16_f32 v161, v136, v140\n\t"
    "v_cvt_pk_bf16_f32 v165, v137, v141\n\t"
    "v_cvt_pk_bf16_f32 v169, v138, v142\n\t"
    "v_cvt_pk_bf16_f32 v173, v139, v143\n\t"
    "s_waitcnt vmcnt(3)\n\t"
    "v_cvt_f32_i32 v144, v144\n\t"
    "v_cvt_f32_i32 v145, v145\n\t"
    "v_cvt_f32_i32 v146, v146\n\t"
    "v_cvt_f32_i32 v147, v147\n\t"
    "v_cvt_f32_i32 v148, v148\n\t"
    "v_cvt_f32_i32 v149, v149\n\t"
    "v_cvt_f32_i32 v150, v150\n\t"
    "v_cvt_f32_i32 v151, v151\n\t"
    "v_fma_f32 v144, v144, v176, v180\n\t"
    "v_fma_f32 v145, v145, v177, v181\n\t"
    "v_fma_f32 v146, v146, v178, v182\n\t"
    "v_fma_f32 v147, v147, v179, v183\n\t"
    "v_fma_f32 v148, v148, v176, v180\n\t"
    "v_fma_f32 v149, v149, v177, v181\n\t"
    "v_fma_f32 v150, v150, v178, v182\n\t"
    "v_fma_f32 v151, v151, v179, v183\n\t"
    "v_mul_f32 v144, v144, v188\n\t"
    "v_mul_f32 v145, v145, v188\n\t"
    "v_mul_f32 v146, v146, v188\n\t"
    "v_mul_f32 v147, v147, v188\n\t"
    "v_mul_f32 v148, v148, v189\n\t"
    "v_mul_f32 v149, v149, v189\n\t"
    "v_mul_f32 v150, v150, v189\n\t"
    "v_mul_f32 v151, v151, v189\n\t"
    "v_cvt_pk_bf16_f32 v162, v144, v148\n\t"
    "v_cvt_pk_bf16_f32 v166, v145, v149\n\t"
    "v_cvt_pk_bf16_f32 v170, v146, v150\n\t"
    "v_cvt_pk_bf16_f32 v174, v147, v151\n\t"
    "s_waitcnt vmcnt(1)\n\t"
    "v_cvt_f32_i32 v152, v152\n\t"
    "v_cvt_f32_i32 v153, v153\n\t"
    "v_cvt_f32_i32 v154, v154\n\t"
    "v_cvt_f32_i32 v155, v155\n\t"
    "v_cvt_f32_i32 v156, v156\n\t"
    "v_cvt_f32_i32 v157, v157\n\t"
    "v_cvt_f32_i32 v158, v158\n\t"
    "v_cvt_f32_i32 v159, v159\n\t"
    "v_fma_f32 v152, v152, v176, v180\n\t"
    "v_fma_f32 v153, v153, v177, v181\n\t"
    "v_fma_f32 v154, v154, v178, v182\n\t"
    "v_fma_f32 v155, v155, v179, v183\n\t"
    "v_fma_f32 v156, v156, v176, v180\n\t"
    "v_fma_f32 v157, v157, v177, v181\n\t"
    "v_fma_f32 v158, v158, v178, v182\n\t"
    "v_fma_f32 v159, v159, v179, v183\n\t"
    "v_mul_f32 v152, v152, v190\n\t"
    "v_mul_f32 v153, v153, v190\n\t"
    "v_mul_f32 v154, v154, v190\n\t"
    "v_mul_f32 v155, v155, v190\n\t"
    "v_mul_f32 v156, v156, v191\n\t"
    "v_mul_f32 v157, v157, v191\n\t"
    "v_mul_f32 v158, v158, v191\n\t"
    "v_mul_f32 v159, v159, v191\n\t"
    "v_cvt_pk_bf16_f32 v163, v152, v156\n\t"
    "v_cvt_pk_bf16_f32 v167, v153, v157\n\t"
    "v_cvt_pk_bf16_f32 v171, v154, v158\n\t"
    "v_cvt_pk_bf16_f32 v175, v155, v159\n\t"
    "buffer_store_dwordx4 v[160:163], v240, s[44:47], s62 offen offset:0 sc1\n\t"
    "buffer_store_dwordx4 v[164:167], v240, s[44:47], s62 offen offset:512 sc1\n\t"
    "buffer_store_dwordx4 v[168:171], v240, s[44:47], s62 offen offset:1024 sc1\n\t"
    "buffer_store_dwordx4 v[172:175], v240, s[44:47], s62 offen offset:1536 sc1\n\t"
    "s_mov_b32 s14, s64\n\t"
    "s_mul_i32 s54, s64, 18725\n\t"
    "s_lshr_b32 s54, s54, 21\n\t"
    "s_mul_i32 s55, s54, 112\n\t"
    "s_sub_u32 s55, s64, s55\n\t"
    "s_lshl_b32 s54, s54, 1\n\t"
    "s_add_u32 s54, s54, 5\n\t"
    "s_and_b32 s56, s55, 3\n\t"
    "s_lshr_b32 s57, s55, 2\n\t"
    "s_lshl_b32 s93, s55, 6\n\t"
    "s_lshl_b32 s12, s54, 2\n\t"
    "s_add_u32 s93, s93, s12\n\t"
    "s_add_u32 s12, s12, s56\n\t"
    "s_mul_i32 s59, s12, 229376\n\t"
    "s_lshl_b32 s13, s57, 9\n\t"
    "s_add_u32 s59, s59, s13\n\t"
    "s_lshl_b32 s61, s12, 5\n\t"
    "s_lshr_b32 s12, s12, 1\n\t"
    "s_mul_i32 s60, s12, 7168\n\t"
    "s_lshl_b32 s13, s57, 8\n\t"
    "s_add_u32 s60, s60, s13\n\t"
    "s_lshl_b32 s12, s57, 4\n\t"
    "s_add_u32 s12, s12, s54\n\t"
    "s_lshl_b32 s12, s12, 2\n\t"
    "s_add_u32 s12, s12, s56\n\t"
    "s_lshl_b32 s62, s12, 12\n\t"
    "s_add_u32 s62, s62, 65536\n\t"
    "s_cmp_lt_u32 s64, 672\n\t"
    "s_cselect_b32 s22, s22, 0\n\t"
    "s_cselect_b32 s26, s26, 0\n\t"
    "s_cselect_b32 s30, s30, 0\n\t"
    "s_cselect_b32 s38, s38, 0\n\t"
    "s_cselect_b32 s50, s50, 0\n\t"
    "s_add_u32 s64, s64, 224\n\t"
    "buffer_load_dword v213, v242, s[48:51], s93 offen sc1\n\t"
    "buffer_load_dwordx2 v[192:193], v208, s[24:27], s60 offen\n\t"
    "buffer_load_dwordx2 v[194:195], v208, s[28:31], s60 offen\n\t"
    "buffer_load_dwordx4 v[196:199], v209, s[36:39], s61 offen\n\t"
    "buffer_load_dwordx4 v[128:131], v200, s[20:23], s59 offen nt\n\t"
    "buffer_load_dwordx4 v[132:135], v201, s[20:23], s59 offen nt\n\t"
    "buffer_load_dwordx4 v[136:139], v202, s[20:23], s59 offen nt\n\t"
    "buffer_load_dwordx4 v[140:143], v203, s[20:23], s59 offen nt\n\t"
    "buffer_load_dwordx4 v[144:147], v204, s[20:23], s59 offen nt\n\t"
    "buffer_load_dwordx4 v[148:151], v205, s[20:23], s59 offen nt\n\t"
    "buffer_load_dwordx4 v[152:155], v206, s[20:23], s59 offen nt\n\t"
    "buffer_load_dwordx4 v[156:159], v207, s[20:23], s59 offen nt\n\t"
    "s_waitcnt vmcnt(12)\n\t"
    "s_mov_b64 exec, 1\n\t"
    "s_cmp_eq_u32 s96, 0\n\t"
    "s_cbranch_scc1 Lhp1%=\n\t"
    "buffer_store_dword v8, v242, s[44:47], s95 offen sc1\n\t"
    "Lhp1%=:\n\t"
    "buffer_store_dword v241, v242, s[44:47], s94 offen sc1\n\t"
    "v_add_u32 v8, 1, v241\n\t"
    "s_mov_b32 s95, s94\n\t"
    "s_mov_b32 s96, 1\n\t"
    "Lhp2%=:\n\t"
    "s_mov_b64 exec, -1\n\t"
    "s_cmp_lt_u32 s14, 672\n\t"
    "s_cbranch_scc1 Lht%=\n\t"
    "Lend%=:\n\t"
    ::"s"(ka),"s"(blockIdx.x),"s"(w),"s"(ldsa)
    :"v0","v1","v2","v3","v4","v5","v6","v7","v8","v9","v10","v11","v12","v13","v14","v15","v16","v17","v18","v19","v20","v21","v22","v23","v24","v25","v26","v27","v28","v29","v30","v31","v32","v33","v34","v35","v36","v37","v38","v39","v40","v41","v42","v43","v44","v45","v46","v47","v48","v49","v50","v51","v52","v53","v54","v55","v56","v57","v58","v59","v60","v61","v62","v63","v64","v65","v66","v67","v68","v69","v70","v71","v72","v73","v74","v75","v76","v77","v78","v79","v80","v81","v82","v83","v84","v85","v86","v87","v88","v89","v90","v91","v92","v93","v94","v95","v96","v97","v98","v99","v100","v101","v102","v103","v104","v105","v106","v107","v108","v109","v110","v111","v112","v113","v114","v115","v116","v117","v118","v119","v120","v121","v122","v123","v124","v125","v126","v127","v128","v129","v130","v131","v132","v133","v134","v135","v136","v137","v138","v139","v140","v141","v142","v143","v144","v145","v146","v147","v148","v149","v150","v151","v152","v153","v154","v155","v156","v157","v158","v159","v160","v161","v162","v163","v164","v165","v166","v167","v168","v169","v170","v171","v172","v173","v174","v175","v176","v177","v178","v179","v180","v181","v182","v183","v184","v185","v186","v187","v188","v189","v190","v191","v192","v193","v194","v195","v196","v197","v198","v199","v200","v201","v202","v203","v204","v205","v206","v207","v208","v209","v210","v211","v212","v213","v214","v215","v216","v217","v218","v219","v220","v221","v222","v223","v224","v225","v226","v227","v228","v229","v230","v231","v232","v233","v234","v235","v236","v237","v238","v239","v240","v241","v242","v243","v244","v245","v246","v247","v248","v249","v250","v251","v252","v253","v254","v255","s12","s13","s14","s15","s16","s17","s18","s19","s20","s21","s22","s23","s24","s25","s26","s27","s28","s29","s30","s31","s36","s37","s38","s39","s40","s41","s42","s43","s44","s45","s46","s47","s48","s49","s50","s51","s52","s53","s54","s55","s56","s57","s58","s59","s60","s61","s62","s63","s64","s65","s66","s67","s68","s69","s70","s71","s72","s73","s74","s75","s76","s77","s78","s79","s80","s81","s82","s83","s84","s85","s86","s87","s88","s89","s90","s91","s92","s93","s94","s95","s96","s97","vcc","scc","memory");
}

#define WQ_LAUNCH(...) wq_gemm<<<256,512,0,stream>>>(__VA_ARGS__)
extern "C" void kernel_launch(void* const* d_in,const int* in_sizes,int n_in,void* d_out,int out_size,void* d_ws,size_t ws_size,hipStream_t stream){
  WQ_LAUNCH((const u16*)d_in[0],(const int*)d_in[1],(const u16*)d_in[2],(const u16*)d_in[3],(const u16*)d_in[4],(u16*)d_out,(unsigned*)((char*)d_ws+(0)));
}
```
